```python
import torch

def _softmax(x, axis=-1):
    return torch.softmax(x, dim=axis)
def _gelu(x):
    return torch.nn.functional.gelu(x, approximate="tanh")
def _silu(x):
    return torch.nn.functional.silu(x)
def _sigmoid(x):
    return torch.sigmoid(x)
def _layernorm(x, axis=-1, eps=1e-5):
    m = x.mean(dim=axis, keepdim=True)
    v = x.var(dim=axis, keepdim=True, unbiased=False)
    return (x - m) / torch.sqrt(v + eps)
def _scatter_add(tgt, idx, src, axis=0):
    return tgt.clone().index_add_(axis, idx, src)
def _segment_sum(src, seg, n):
    out = torch.zeros((n,) + src.shape[1:], dtype=src.dtype)
    return out.index_add_(0, seg, src)
def _pad(x, pw):
    flat = tuple(v for pair in reversed(pw) for v in pair)
    return torch.nn.functional.pad(x, flat)

kernel_name = 'sequential_preLN__sliding_local_dilated__dense_activation_silu__prompt'

def setup_inputs():

    _w = (lambda a: (lambda t: t.to(torch.bfloat16) if t.is_floating_point() else t)(torch.as_tensor(a)))
    def _A_setup():
        B, S, D = 1, 2048, 640
        return {
            "x": _w((lambda *s: torch.randn(*s, dtype=torch.bfloat16) * 0.05)(B, S, D)),
            "wqkv": _w((lambda *s: torch.randn(*s, dtype=torch.bfloat16) * 0.05)(D, 3 * D)),
            "wo": _w((lambda *s: torch.randn(*s, dtype=torch.bfloat16) * 0.05)(D, D)),
        }
    _A_in = _A_setup()

    def _B_setup():
        B, S, D, F = 1, 2048, 640, 1728
        return {
            "x": _w((lambda *s: torch.randn(*s, dtype=torch.bfloat16) * 0.05)(B, S, D)),
            "w1": _w((lambda *s: torch.randn(*s, dtype=torch.bfloat16) * 3.2)(D, F) * (D ** -0.5)),
            "w2": _w((lambda *s: torch.randn(*s, dtype=torch.bfloat16) * 0.4)(F, D) * (F ** -0.5)),
        }
    _B_in = _B_setup()

    return {
        "A_x": _A_in["x"],
        "A_wqkv": _A_in["wqkv"],
        "A_wo": _A_in["wo"],
        "B_x": _B_in["x"],
        "B_w1": _B_in["w1"],
        "B_w2": _B_in["w2"],
    }

def reference(A_x, A_wqkv, A_wo, B_x, B_w1, B_w2):
    def _A_ref(x, wqkv, wo):
        B, S, D = x.shape
        H = 32; Dh = D // H
        W, dil = 8, 2
        qkv = (lambda x, *d: x.permute(*d))((x @ wqkv).reshape(B, S, 3, H, Dh), 2, 0, 3, 1, 4)
        q, k, v = qkv[0], qkv[1], qkv[2]
        scale = Dh ** -0.5
        pos = torch.arange(S)
        off = torch.arange(W) * dil
        idx = pos[:, None] - off[None, :]
        mask = idx >= 0
        idx_c = torch.where(mask, idx, 0).reshape(-1)
        kg = k[:, :, idx_c, :].reshape(B, H, S, W, Dh)
        vg = v[:, :, idx_c, :].reshape(B, H, S, W, Dh)
        logits = torch.einsum("bhsd,bhswd->bhsw", q, kg) * scale
        logits = torch.where(mask[None, None, :, :], logits, -1e9)
        attn = _softmax(logits, axis=-1)
        o = torch.einsum("bhsw,bhswd->bhsd", attn, vg)
        return (lambda x, *d: x.permute(*d))(o, 0, 2, 1, 3).reshape(B, S, D) @ wo

    def _B_ref(x, w1, w2):
        h = torch.einsum("bsd,df->bsf", x, w1)
        h = _silu(h)
        out = torch.einsum("bsf,fd->bsd", h, w2)
        return out

    x = A_x
    h = x + _A_ref(_layernorm(x, axis=-1), A_wqkv, A_wo)
    h2 = h + _B_ref(_layernorm(h, axis=-1), B_w1, B_w2)
    return h2
```

```cpp
#include <hip/hip_runtime.h>
#include <hip/hip_bf16.h>
typedef __hip_bfloat16 bf16;
typedef short bf16x8 __attribute__((ext_vector_type(8)));
typedef float f32x4 __attribute__((ext_vector_type(4)));
constexpr int S=2048,D=640,F=1728,H=32,DH=20;

static __device__ __forceinline__ float b2f(unsigned short b){ return __uint_as_float(((unsigned)b)<<16); }
static __device__ __forceinline__ unsigned short f2b(float f){ bf16 h=__float2bfloat16(f); return *(unsigned short*)&h; }

__global__ void ln_k(const unsigned short* x, unsigned short* y){
  int row=blockIdx.x*4+(threadIdx.x>>6), lane=threadIdx.x&63;
  float v[10]; float s=0;
  for(int i=0;i<10;i++){ v[i]=b2f(x[row*D+lane+64*i]); s+=v[i]; }
  for(int o=32;o;o>>=1) s+=__shfl_xor(s,o);
  float m=s*(1.f/D); float q=0;
  for(int i=0;i<10;i++){ float d=v[i]-m; q+=d*d; }
  for(int o=32;o;o>>=1) q+=__shfl_xor(q,o);
  float r=rsqrtf(q*(1.f/D)+1e-5f);
  for(int i=0;i<10;i++) y[row*D+lane+64*i]=f2b((v[i]-m)*r);
}
template<int EPI>
__global__ void gemm_k(const unsigned short* A,const unsigned short* B,unsigned short* C,const unsigned short* R,int N,int K){
  __shared__ unsigned short As[64][40], Bs[64][40];
  int bm=blockIdx.y*64,bn=blockIdx.x*64,t=threadIdx.x,w=t>>6,l=t&63;
  int wr=w>>1,wc=w&1;
  f32x4 acc[2][2]={};
  for(int k0=0;k0<K;k0+=32){
    { int r=t>>2,c=(t&3)*8; *(bf16x8*)&As[r][c]=*(const bf16x8*)&A[(size_t)(bm+r)*K+k0+c]; }
    { int k=t>>3,c=(t&7)*8; bf16x8 v=*(const bf16x8*)&B[(size_t)(k0+k)*N+bn+c]; for(int j=0;j<8;j++) Bs[c+j][k]=v[j]; }
    __syncthreads();
    bf16x8 a[2],b[2];
    for(int i=0;i<2;i++){ a[i]=*(bf16x8*)&As[wr*32+i*16+(l&15)][(l>>4)*8]; b[i]=*(bf16x8*)&Bs[wc*32+i*16+(l&15)][(l>>4)*8]; }
    for(int i=0;i<2;i++)for(int j=0;j<2;j++) acc[i][j]=__builtin_amdgcn_mfma_f32_16x16x32_bf16(a[i],b[j],acc[i][j],0,0,0);
    __syncthreads();
  }
  for(int i=0;i<2;i++)for(int j=0;j<2;j++)for(int r=0;r<4;r++){
    int row=bm+wr*32+i*16+(l>>4)*4+r, col=bn+wc*32+j*16+(l&15);
    float v=acc[i][j][r];
    if(EPI==1){ v=v/(1.f+__expf(-v)); }
    if(EPI==2){ v=b2f(f2b(v))+b2f(R[(size_t)row*N+col]); }
    C[(size_t)row*N+col]=f2b(v);
  }
}
__global__ void attn_k(const unsigned short* qkv, unsigned short* o){
  int idx=blockIdx.x*blockDim.x+threadIdx.x; int s=idx/H,h=idx%H;
  float q[DH]; for(int d=0;d<DH;d++) q[d]=b2f(qkv[(size_t)s*3*D+h*DH+d]);
  float lg[8]; float mx=-1e30f;
  for(int w=0;w<8;w++){ int p=s-2*w; float a=-1e9f; if(p>=0){ a=0; for(int d=0;d<DH;d++) a+=q[d]*b2f(qkv[(size_t)p*3*D+D+h*DH+d]); a*=0.22360679774997896f;} lg[w]=a; mx=fmaxf(mx,a);}
  float sum=0; for(int w=0;w<8;w++){ lg[w]=__expf(lg[w]-mx); sum+=lg[w]; }
  float inv=1.f/sum; float acc[DH]; for(int d=0;d<DH;d++)acc[d]=0;
  for(int w=0;w<8;w++){ int p=s-2*w; if(p<0)p=0; float a=lg[w]*inv; for(int d=0;d<DH;d++) acc[d]+=a*b2f(qkv[(size_t)p*3*D+2*D+h*DH+d]); }
  for(int d=0;d<DH;d++) o[(size_t)s*D+h*DH+d]=f2b(acc[d]);
}
extern "C" void kernel_launch(void* const* d_in, const int* in_sizes, int n_in, void* d_out, int out_size, void* d_ws, size_t ws_size, hipStream_t stream){
  const unsigned short* x=(const unsigned short*)d_in[0],*wqkv=(const unsigned short*)d_in[1],*wo=(const unsigned short*)d_in[2],*w1=(const unsigned short*)d_in[4],*w2=(const unsigned short*)d_in[5];
  unsigned short* ws=(unsigned short*)d_ws; unsigned short* xn=ws,*qkv=xn+S*D,*o=qkv+S*3*D,*h=o+S*D,*hn=h+S*D,*hh=hn+S*D;
  ln_k<<<S/4,256,0,stream>>>(x,xn);
  gemm_k<0><<<dim3(3*D/64,S/64),256,0,stream>>>(xn,wqkv,qkv,nullptr,3*D,D);
  attn_k<<<S*H/256,256,0,stream>>>(qkv,o);
  gemm_k<2><<<dim3(D/64,S/64),256,0,stream>>>(o,wo,h,x,D,D);
  ln_k<<<S/4,256,0,stream>>>(h,hn);
  gemm_k<1><<<dim3(F/64,S/64),256,0,stream>>>(hn,w1,hh,nullptr,F,D);
  gemm_k<2><<<dim3(D/64,S/64),256,0,stream>>>(hh,w2,(unsigned short*)d_out,h,D,F);
}
```

```cpp
#include <hip/hip_runtime.h>
#include <hip/hip_bf16.h>
typedef __hip_bfloat16 bf16;
typedef short bf16x8 __attribute__((ext_vector_type(8)));
typedef float f32x4 __attribute__((ext_vector_type(4)));
constexpr int S=2048,D=640,F=1728,H=32,DH=20;

static __device__ __forceinline__ float b2f(unsigned short b){ return __uint_as_float(((unsigned)b)<<16); }
static __device__ __forceinline__ unsigned short f2b(float f){ bf16 h=__float2bfloat16(f); return *(unsigned short*)&h; }

__global__ void ln_k(const unsigned short* x, unsigned short* y){
  int row=blockIdx.x*4+(threadIdx.x>>6), lane=threadIdx.x&63;
  float v[10]; float s=0;
  for(int i=0;i<10;i++){ v[i]=b2f(x[row*D+lane+64*i]); s+=v[i]; }
  for(int o=32;o;o>>=1) s+=__shfl_xor(s,o);
  float m=s*(1.f/D); float q=0;
  for(int i=0;i<10;i++){ float d=v[i]-m; q+=d*d; }
  for(int o=32;o;o>>=1) q+=__shfl_xor(q,o);
  float r=rsqrtf(q*(1.f/D)+1e-5f);
  for(int i=0;i<10;i++) y[row*D+lane+64*i]=f2b((v[i]-m)*r);
}
template<int EPI>
__global__ void gemm_k(const unsigned short* A,const unsigned short* B,unsigned short* C,const unsigned short* R,int N,int K){
  __shared__ unsigned short As[64][40], Bs[64][40];
  int bm=blockIdx.y*64,bn=blockIdx.x*64,t=threadIdx.x,w=t>>6,l=t&63;
  int wr=w>>1,wc=w&1;
  f32x4 acc[2][2]={};
  for(int k0=0;k0<K;k0+=32){
    { int r=t>>2,c=(t&3)*8; *(bf16x8*)&As[r][c]=*(const bf16x8*)&A[(size_t)(bm+r)*K+k0+c]; }
    { int k=t>>3,c=(t&7)*8; bf16x8 v=*(const bf16x8*)&B[(size_t)(k0+k)*N+bn+c]; for(int j=0;j<8;j++) Bs[c+j][k]=v[j]; }
    __syncthreads();
    bf16x8 a[2],b[2];
    for(int i=0;i<2;i++){ a[i]=*(bf16x8*)&As[wr*32+i*16+(l&15)][(l>>4)*8]; b[i]=*(bf16x8*)&Bs[wc*32+i*16+(l&15)][(l>>4)*8]; }
    for(int i=0;i<2;i++)for(int j=0;j<2;j++) acc[i][j]=__builtin_amdgcn_mfma_f32_16x16x32_bf16(a[i],b[j],acc[i][j],0,0,0);
    __syncthreads();
  }
  for(int i=0;i<2;i++)for(int j=0;j<2;j++)for(int r=0;r<4;r++){
    int row=bm+wr*32+i*16+(l>>4)*4+r, col=bn+wc*32+j*16+(l&15);
    float v=acc[i][j][r];
    if(EPI==1){ v=v/(1.f+__expf(-v)); }
    if(EPI==2){ v=b2f(f2b(v))+b2f(R[(size_t)row*N+col]); }
    C[(size_t)row*N+col]=f2b(v);
  }
}
__global__ void attn_k(const unsigned short* qkv, unsigned short* o){
  int idx=blockIdx.x*blockDim.x+threadIdx.x; int s=idx/H,h=idx%H;
  float q[DH]; for(int d=0;d<DH;d++) q[d]=b2f(qkv[(size_t)s*3*D+h*DH+d]);
  float lg[8]; float mx=-1e30f;
  for(int w=0;w<8;w++){ int p=s-2*w; float a=-1e9f; if(p>=0){ a=0; for(int d=0;d<DH;d++) a+=q[d]*b2f(qkv[(size_t)p*3*D+D+h*DH+d]); a*=0.22360679774997896f;} lg[w]=a; mx=fmaxf(mx,a);}
  float sum=0; for(int w=0;w<8;w++){ lg[w]=__expf(lg[w]-mx); sum+=lg[w]; }
  float inv=1.f/sum; float acc[DH]; for(int d=0;d<DH;d++)acc[d]=0;
  for(int w=0;w<8;w++){ int p=s-2*w; if(p<0)p=0; float a=lg[w]*inv; for(int d=0;d<DH;d++) acc[d]+=a*b2f(qkv[(size_t)p*3*D+2*D+h*DH+d]); }
  for(int d=0;d<DH;d++) o[(size_t)s*D+h*DH+d]=f2b(acc[d]);
}
#define PROBE 0
#define MAXW 0

typedef unsigned v4u __attribute__((ext_vector_type(4)));
__device__ __forceinline__ unsigned ct(){ unsigned long long t; __builtin_amdgcn_sched_barrier(0); asm volatile("s_memtime %0\n\ts_waitcnt lgkmcnt(0)":"=s"(t)::"memory"); __builtin_amdgcn_sched_barrier(0); return (unsigned)t;}
__global__ __launch_bounds__(256) void probe(const char* x,const char* wqkv,const char* w1,const char* w2,char* ws,int AMP){
  __shared__ char pad[96*1024];
  int b=blockIdx.x,t=threadIdx.x,lane=t&63,w=__builtin_amdgcn_readfirstlane(t>>6);
  const char* src = w==0? x : w==1? wqkv : w==2? w1 : w2;
  __amdgpu_buffer_rsrc_t rs=__builtin_amdgcn_make_buffer_rsrc((void*)src,0,2211840,0x00020000);
  __amdgpu_buffer_rsrc_t rw=__builtin_amdgcn_make_buffer_rsrc((void*)ws,0,8<<20,0x00020000);
  unsigned vo=lane*16; unsigned a=t,c=t*3;
  unsigned t0=ct();
  unsigned d=ct()-t0;
  if(d>40000) d=40000;
  if(MAXW || b==77){ unsigned s=ct(); while(ct()-s < d*AMP) __builtin_amdgcn_s_sleep(2); }
  if(a==0x123456) pad[c&1023]=1;
  if(c==0x7654321) ws[0]=pad[a&1023];
}

extern "C" void kernel_launch(void* const* d_in, const int* in_sizes, int n_in, void* d_out, int out_size, void* d_ws, size_t ws_size, hipStream_t stream){
  const unsigned short* x=(const unsigned short*)d_in[0],*wqkv=(const unsigned short*)d_in[1],*wo=(const unsigned short*)d_in[2],*w1=(const unsigned short*)d_in[4],*w2=(const unsigned short*)d_in[5];
  unsigned short* ws=(unsigned short*)d_ws; unsigned short* xn=ws,*qkv=xn+S*D,*o=qkv+S*3*D,*h=o+S*D,*hn=h+S*D,*hh=hn+S*D;
  probe<<<256,256,0,stream>>>((const char*)x,(const char*)wqkv,(const char*)w1,(const char*)w2,(char*)d_ws+(64<<20),100);
  ln_k<<<S/4,256,0,stream>>>(x,xn);
  gemm_k<0><<<dim3(3*D/64,S/64),256,0,stream>>>(xn,wqkv,qkv,nullptr,3*D,D);
  attn_k<<<S*H/256,256,0,stream>>>(qkv,o);
  gemm_k<2><<<dim3(D/64,S/64),256,0,stream>>>(o,wo,h,x,D,D);
  ln_k<<<S/4,256,0,stream>>>(h,hn);
  gemm_k<1><<<dim3(F/64,S/64),256,0,stream>>>(hn,w1,hh,nullptr,F,D);
  gemm_k<2><<<dim3(D/64,S/64),256,0,stream>>>(hh,w2,(unsigned short*)d_out,h,D,F);
}
```

```cpp
#include <hip/hip_runtime.h>
#include <hip/hip_bf16.h>
typedef __hip_bfloat16 bf16;
typedef short bf16x8 __attribute__((ext_vector_type(8)));
typedef float f32x4 __attribute__((ext_vector_type(4)));
constexpr int S=2048,D=640,F=1728,H=32,DH=20;

static __device__ __forceinline__ float b2f(unsigned short b){ return __uint_as_float(((unsigned)b)<<16); }
static __device__ __forceinline__ unsigned short f2b(float f){ bf16 h=__float2bfloat16(f); return *(unsigned short*)&h; }

__global__ void ln_k(const unsigned short* x, unsigned short* y){
  int row=blockIdx.x*4+(threadIdx.x>>6), lane=threadIdx.x&63;
  float v[10]; float s=0;
  for(int i=0;i<10;i++){ v[i]=b2f(x[row*D+lane+64*i]); s+=v[i]; }
  for(int o=32;o;o>>=1) s+=__shfl_xor(s,o);
  float m=s*(1.f/D); float q=0;
  for(int i=0;i<10;i++){ float d=v[i]-m; q+=d*d; }
  for(int o=32;o;o>>=1) q+=__shfl_xor(q,o);
  float r=rsqrtf(q*(1.f/D)+1e-5f);
  for(int i=0;i<10;i++) y[row*D+lane+64*i]=f2b((v[i]-m)*r);
}
template<int EPI>
__global__ void gemm_k(const unsigned short* A,const unsigned short* B,unsigned short* C,const unsigned short* R,int N,int K){
  __shared__ unsigned short As[64][40], Bs[64][40];
  int bm=blockIdx.y*64,bn=blockIdx.x*64,t=threadIdx.x,w=t>>6,l=t&63;
  int wr=w>>1,wc=w&1;
  f32x4 acc[2][2]={};
  for(int k0=0;k0<K;k0+=32){
    { int r=t>>2,c=(t&3)*8; *(bf16x8*)&As[r][c]=*(const bf16x8*)&A[(size_t)(bm+r)*K+k0+c]; }
    { int k=t>>3,c=(t&7)*8; bf16x8 v=*(const bf16x8*)&B[(size_t)(k0+k)*N+bn+c]; for(int j=0;j<8;j++) Bs[c+j][k]=v[j]; }
    __syncthreads();
    bf16x8 a[2],b[2];
    for(int i=0;i<2;i++){ a[i]=*(bf16x8*)&As[wr*32+i*16+(l&15)][(l>>4)*8]; b[i]=*(bf16x8*)&Bs[wc*32+i*16+(l&15)][(l>>4)*8]; }
    for(int i=0;i<2;i++)for(int j=0;j<2;j++) acc[i][j]=__builtin_amdgcn_mfma_f32_16x16x32_bf16(a[i],b[j],acc[i][j],0,0,0);
    __syncthreads();
  }
  for(int i=0;i<2;i++)for(int j=0;j<2;j++)for(int r=0;r<4;r++){
    int row=bm+wr*32+i*16+(l>>4)*4+r, col=bn+wc*32+j*16+(l&15);
    float v=acc[i][j][r];
    if(EPI==1){ v=v/(1.f+__expf(-v)); }
    if(EPI==2){ v=b2f(f2b(v))+b2f(R[(size_t)row*N+col]); }
    C[(size_t)row*N+col]=f2b(v);
  }
}
__global__ void attn_k(const unsigned short* qkv, unsigned short* o){
  int idx=blockIdx.x*blockDim.x+threadIdx.x; int s=idx/H,h=idx%H;
  float q[DH]; for(int d=0;d<DH;d++) q[d]=b2f(qkv[(size_t)s*3*D+h*DH+d]);
  float lg[8]; float mx=-1e30f;
  for(int w=0;w<8;w++){ int p=s-2*w; float a=-1e9f; if(p>=0){ a=0; for(int d=0;d<DH;d++) a+=q[d]*b2f(qkv[(size_t)p*3*D+D+h*DH+d]); a*=0.22360679774997896f;} lg[w]=a; mx=fmaxf(mx,a);}
  float sum=0; for(int w=0;w<8;w++){ lg[w]=__expf(lg[w]-mx); sum+=lg[w]; }
  float inv=1.f/sum; float acc[DH]; for(int d=0;d<DH;d++)acc[d]=0;
  for(int w=0;w<8;w++){ int p=s-2*w; if(p<0)p=0; float a=lg[w]*inv; for(int d=0;d<DH;d++) acc[d]+=a*b2f(qkv[(size_t)p*3*D+2*D+h*DH+d]); }
  for(int d=0;d<DH;d++) o[(size_t)s*D+h*DH+d]=f2b(acc[d]);
}
#define PROBE 4

typedef unsigned v4u __attribute__((ext_vector_type(4)));
__device__ __forceinline__ unsigned ct(){ unsigned long long t; __builtin_amdgcn_sched_barrier(0); asm volatile("s_memtime %0\n\ts_waitcnt lgkmcnt(0)":"=s"(t)::"memory"); __builtin_amdgcn_sched_barrier(0); return (unsigned)t;}
__device__ __forceinline__ unsigned rt(){ unsigned long long t; __builtin_amdgcn_sched_barrier(0); asm volatile("s_memrealtime %0\n\ts_waitcnt lgkmcnt(0)":"=s"(t)::"memory"); __builtin_amdgcn_sched_barrier(0); return (unsigned)t;}
__global__ __launch_bounds__(256) void probe(const char* x,const char* wqkv,const char* w1,const char* w2,char* ws,int AMP_){
  __shared__ char pad[96*1024];
  const int AMP=100;
  int b=blockIdx.x,t=threadIdx.x; unsigned a=t,c=t*3; unsigned d=0;
  { unsigned t0=ct();
    asm volatile(".rept 128\nv_add3_u32 %0,%0,%1,%1\n.endr\n":"+v"(a):"v"(c));
    d=ct()-t0-512; }
  if(d>40000) d=40000;
  if(b==77){ unsigned s=ct(); while(ct()-s < d*AMP) __builtin_amdgcn_s_sleep(2); }
  if(a==0x123456) pad[c&1023]=1;
  if(c==0x7654321) asm volatile(""::"v"((int)pad[a&1023]));
}

extern "C" void kernel_launch(void* const* d_in, const int* in_sizes, int n_in, void* d_out, int out_size, void* d_ws, size_t ws_size, hipStream_t stream){
  const unsigned short* x=(const unsigned short*)d_in[0],*wqkv=(const unsigned short*)d_in[1],*wo=(const unsigned short*)d_in[2],*w1=(const unsigned short*)d_in[4],*w2=(const unsigned short*)d_in[5];
  unsigned short* ws=(unsigned short*)d_ws; unsigned short* xn=ws,*qkv=xn+S*D,*o=qkv+S*3*D,*h=o+S*D,*hn=h+S*D,*hh=hn+S*D;
  probe<<<256,256,0,stream>>>((const char*)x,(const char*)wqkv,(const char*)w1,(const char*)w2,(char*)d_ws,100);
  ln_k<<<S/4,256,0,stream>>>(x,xn);
  gemm_k<0><<<dim3(3*D/64,S/64),256,0,stream>>>(xn,wqkv,qkv,nullptr,3*D,D);
  attn_k<<<S*H/256,256,0,stream>>>(qkv,o);
  gemm_k<2><<<dim3(D/64,S/64),256,0,stream>>>(o,wo,h,x,D,D);
  ln_k<<<S/4,256,0,stream>>>(h,hn);
  gemm_k<1><<<dim3(F/64,S/64),256,0,stream>>>(hn,w1,hh,nullptr,F,D);
  gemm_k<2><<<dim3(D/64,S/64),256,0,stream>>>(hh,w2,(unsigned short*)d_out,h,D,F);
}
```

```cpp
#include <hip/hip_runtime.h>
#include <hip/hip_bf16.h>
typedef __hip_bfloat16 bf16;
typedef short bf16x8 __attribute__((ext_vector_type(8)));
typedef float f32x4 __attribute__((ext_vector_type(4)));
constexpr int S=2048,D=640,F=1728,H=32,DH=20;

static __device__ __forceinline__ float b2f(unsigned short b){ return __uint_as_float(((unsigned)b)<<16); }
static __device__ __forceinline__ unsigned short f2b(float f){ bf16 h=__float2bfloat16(f); return *(unsigned short*)&h; }

__global__ void ln_k(const unsigned short* x, unsigned short* y){
  int row=blockIdx.x*4+(threadIdx.x>>6), lane=threadIdx.x&63;
  float v[10]; float s=0;
  for(int i=0;i<10;i++){ v[i]=b2f(x[row*D+lane+64*i]); s+=v[i]; }
  for(int o=32;o;o>>=1) s+=__shfl_xor(s,o);
  float m=s*(1.f/D); float q=0;
  for(int i=0;i<10;i++){ float d=v[i]-m; q+=d*d; }
  for(int o=32;o;o>>=1) q+=__shfl_xor(q,o);
  float r=rsqrtf(q*(1.f/D)+1e-5f);
  for(int i=0;i<10;i++) y[row*D+lane+64*i]=f2b((v[i]-m)*r);
}
template<int EPI>
__global__ void gemm_k(const unsigned short* A,const unsigned short* B,unsigned short* C,const unsigned short* R,int N,int K){
  __shared__ unsigned short As[64][40], Bs[64][40];
  int bm=blockIdx.y*64,bn=blockIdx.x*64,t=threadIdx.x,w=t>>6,l=t&63;
  int wr=w>>1,wc=w&1;
  f32x4 acc[2][2]={};
  for(int k0=0;k0<K;k0+=32){
    { int r=t>>2,c=(t&3)*8; *(bf16x8*)&As[r][c]=*(const bf16x8*)&A[(size_t)(bm+r)*K+k0+c]; }
    { int k=t>>3,c=(t&7)*8; bf16x8 v=*(const bf16x8*)&B[(size_t)(k0+k)*N+bn+c]; for(int j=0;j<8;j++) Bs[c+j][k]=v[j]; }
    __syncthreads();
    bf16x8 a[2],b[2];
    for(int i=0;i<2;i++){ a[i]=*(bf16x8*)&As[wr*32+i*16+(l&15)][(l>>4)*8]; b[i]=*(bf16x8*)&Bs[wc*32+i*16+(l&15)][(l>>4)*8]; }
    for(int i=0;i<2;i++)for(int j=0;j<2;j++) acc[i][j]=__builtin_amdgcn_mfma_f32_16x16x32_bf16(a[i],b[j],acc[i][j],0,0,0);
    __syncthreads();
  }
  for(int i=0;i<2;i++)for(int j=0;j<2;j++)for(int r=0;r<4;r++){
    int row=bm+wr*32+i*16+(l>>4)*4+r, col=bn+wc*32+j*16+(l&15);
    float v=acc[i][j][r];
    if(EPI==1){ v=v/(1.f+__expf(-v)); }
    if(EPI==2){ v=b2f(f2b(v))+b2f(R[(size_t)row*N+col]); }
    C[(size_t)row*N+col]=f2b(v);
  }
}
__global__ void attn_k(const unsigned short* qkv, unsigned short* o){
  int idx=blockIdx.x*blockDim.x+threadIdx.x; int s=idx/H,h=idx%H;
  float q[DH]; for(int d=0;d<DH;d++) q[d]=b2f(qkv[(size_t)s*3*D+h*DH+d]);
  float lg[8]; float mx=-1e30f;
  for(int w=0;w<8;w++){ int p=s-2*w; float a=-1e9f; if(p>=0){ a=0; for(int d=0;d<DH;d++) a+=q[d]*b2f(qkv[(size_t)p*3*D+D+h*DH+d]); a*=0.22360679774997896f;} lg[w]=a; mx=fmaxf(mx,a);}
  float sum=0; for(int w=0;w<8;w++){ lg[w]=__expf(lg[w]-mx); sum+=lg[w]; }
  float inv=1.f/sum; float acc[DH]; for(int d=0;d<DH;d++)acc[d]=0;
  for(int w=0;w<8;w++){ int p=s-2*w; if(p<0)p=0; float a=lg[w]*inv; for(int d=0;d<DH;d++) acc[d]+=a*b2f(qkv[(size_t)p*3*D+2*D+h*DH+d]); }
  for(int d=0;d<DH;d++) o[(size_t)s*D+h*DH+d]=f2b(acc[d]);
}
#define WG 77

__device__ __forceinline__ unsigned rt(){ unsigned long long t; asm volatile("s_memrealtime %0\n\ts_waitcnt lgkmcnt(0)":"=s"(t)::"memory"); return (unsigned)t;}
template<int LDS,int BIGV>
__global__ __launch_bounds__(256) void kfirst(unsigned* p){
  __shared__ char pad[LDS];
  unsigned t=rt();
  if(BIGV) asm volatile("v_mov_b32 v255, 0\n v_accvgpr_write_b32 a255, 0":::"v255","a255");
  asm volatile(""::"s"((unsigned)(size_t)(__attribute__((address_space(3))) char*)pad));
  if(blockIdx.x==WG && threadIdx.x==0) p[0]=t;
}
template<int LDS,int BIGV>
__global__ __launch_bounds__(256) void klast(unsigned* p){
  __shared__ char pad[LDS];
  if(BIGV) asm volatile("v_mov_b32 v255, 0\n v_accvgpr_write_b32 a255, 0":::"v255","a255");
  asm volatile(""::"s"((unsigned)(size_t)(__attribute__((address_space(3))) char*)pad));
  unsigned s=p[0];
  if(blockIdx.x==WG){ for(int i=0;i<1000000;i++){ if(rt()-s>=30000u) break; } }
}

extern "C" void kernel_launch(void* const* d_in, const int* in_sizes, int n_in, void* d_out, int out_size, void* d_ws, size_t ws_size, hipStream_t stream){
  const unsigned short* x=(const unsigned short*)d_in[0],*wqkv=(const unsigned short*)d_in[1],*wo=(const unsigned short*)d_in[2],*w1=(const unsigned short*)d_in[4],*w2=(const unsigned short*)d_in[5];
  unsigned short* ws=(unsigned short*)d_ws; unsigned short* xn=ws,*qkv=xn+S*D,*o=qkv+S*3*D,*h=o+S*D,*hn=h+S*D,*hh=hn+S*D;
  unsigned* pp=(unsigned*)((char*)d_ws+(128<<20));
  kfirst<163840,0><<<256,256,0,stream>>>(pp);
  ln_k<<<S/4,256,0,stream>>>(x,xn);
  gemm_k<0><<<dim3(3*D/64,S/64),256,0,stream>>>(xn,wqkv,qkv,nullptr,3*D,D);
  attn_k<<<S*H/256,256,0,stream>>>(qkv,o);
  gemm_k<2><<<dim3(D/64,S/64),256,0,stream>>>(o,wo,h,x,D,D);
  ln_k<<<S/4,256,0,stream>>>(h,hn);
  gemm_k<1><<<dim3(F/64,S/64),256,0,stream>>>(hn,w1,hh,nullptr,F,D);
  gemm_k<2><<<dim3(D/64,S/64),256,0,stream>>>(hh,w2,(unsigned short*)d_out,h,D,F);
  klast<16,0><<<256,256,0,stream>>>(pp);
}
```

```cpp
#include <hip/hip_runtime.h>
#include <hip/hip_bf16.h>

__global__ __launch_bounds__(256, 1) void fused_block_kernel(const void* x, const void* wqkv, const void* wo, const void* w1,
                                                             const void* w2, void* out, void* ws) {
  __shared__ __attribute__((aligned(16))) char lds[163840];
  unsigned tid = threadIdx.x;
  asm volatile(
    ".Lstart_2_%=:\n"
    "s_load_dwordx8 s[24:31], %1, 0x0\n"
    "s_load_dwordx4 s[32:35], %1, 0x20\n"
    "s_load_dwordx2 s[36:37], %1, 0x30\n"
    "s_mov_b32 s40, %2\n"
    "s_lshr_b32 s42, s40, 3\n"
    "s_getpc_b64 s[38:39]\n"
    "v_and_b32 v1, 15, v0\n"
    "v_min_u32_e64 v1, 8, v1\n"
    "s_mul_i32 s43, s42, 9\n"
    "v_add_lshl_u32 v1, v1, s43, 7\n"
    "v_min_u32 v1, .Lend_1_%=-.Lstart_2_%=-64, v1\n"
    "global_load_dword v5, v1, s[38:39]\n"
    "v_lshrrev_b32 v1, 6, v0\n"
    "v_and_b32 v6, 63, v0\n"
    "v_readfirstlane_b32 s41, v1\n"
    "v_lshlrev_b32 v7, 4, v6\n"
    "v_and_b32 v8, 15, v6\n"
    "v_lshrrev_b32 v9, 4, v6\n"
    "s_and_b32 s47, s40, 1\n"
    "s_lshl_b32 s48, s41, 10\n"
    "s_mov_b32 s7, 0x20000\n"
    "s_mov_b32 s11, 0x20000\n"
    "s_mov_b32 s15, 0x20000\n"
    "s_mov_b32 s19, 0x20000\n"
    "s_mov_b32 s23, 0x20000\n"
    "s_mov_b32 s6, 0x280000\n"
    "s_mov_b32 s10, 0x258000\n"
    "s_mov_b32 s14, 0x1454000\n"
    "s_mov_b32 s18, 0x280000\n"
    "s_bfe_u32 s45, s40, 0x20001\n"
    "s_lshr_b32 s43, s40, 6\n"
    "s_lshl2_add_u32 s49, s45, s43\n"
    "s_and_b32 s44, s42, 7\n"
    "s_lshl3_add_u32 s50, s47, s44\n"
    "s_lshr_b32 s51, s50, 1\n"
    "s_and_b32 s52, s50, 1\n"
    "s_and_b32 s53, s49, 1\n"
    "s_and_b32 s44, s49, 14\n"
    "s_add_u32 s44, s44, s52\n"
    "s_lshl_b32 s54, s44, 6\n"
    "s_lshl1_add_u32 s43, s43, s52\n"
    "s_and_b32 s61, s40, 7\n"
    "s_lshl2_add_u32 s57, s45, s45\n"
    "s_add_u32 s57, s57, s43\n"
    "s_cmp_ge_u32 s43, 5\n"
    "s_cselect_b32 s57, 0xc8, s57\n"
    "s_lshl2_add_u32 s44, s57, s41\n"
    "s_mul_i32 s58, s44, 0x7800\n"
    "s_mul_i32 s44, s51, 0xa0\n"
    "s_and_b32 s44, s44, -128\n"
    "s_add_u32 s58, s58, s44\n"
    "s_mul_i32 s59, s41, 0x1800\n"
    "s_and_b32 s44, s41, 1\n"
    "s_lshl_b32 s44, s44, 3\n"
    "s_lshl_b32 s61, s45, 7\n"
    "v_lshl_add_u32 v1, v9, 1, s44\n"
    "v_xor_b32 v1, v1, v8\n"
    "v_mul_u32_u24 v2, 0xf00, v9\n"
    "v_lshl_add_u32 v10, v1, 4, v2\n"
    "s_waitcnt lgkmcnt(0)\n"
    "s_mov_b32 s8, s26\n"
    "s_and_b32 s9, s27, 0xffff\n"
    "s_add_u32 m0, s59, 0x17000\n"
    "s_nop 0\n"
    "buffer_load_dwordx4 v10, s[8:11], s58 offen lds\n"
    "s_add_u32 s58, s58, 0x3c00\n"
    "s_cmp_ge_u32 s40, 40\n"
    "s_cselect_b64 s[20:21], s[32:33], s[28:29]\n"
    "s_cmp_ge_u32 s40, 0x94\n"
    "s_cselect_b64 s[20:21], s[30:31], s[20:21]\n"
    "s_and_b32 s21, s21, 0xffff\n"
    "s_add_u32 m0, s59, 0x17400\n"
    "s_nop 0\n"
    "buffer_load_dwordx4 v10, s[8:11], s58 offen lds\n"
    "s_add_u32 s58, s58, 0xffffc900\n"
    "s_mov_b32 s12, s36\n"
    "s_and_b32 s13, s37, 0xffff\n"
    "s_mov_b32 s4, s24\n"
    "s_and_b32 s5, s25, 0xffff\n"
    "s_mov_b32 s16, s34\n"
    "s_and_b32 s17, s35, 0xffff\n"
    "s_add_u32 m0, s59, 0x17800\n"
    "s_nop 0\n"
    "buffer_load_dwordx4 v10, s[8:11], s58 offen lds\n"
    "s_add_u32 s58, s58, 0x3c00\n"
    "v_mov_b32_e64 v1, 0\n"
    "buffer_load_dwordx2 v[18:19], v1, s[12:15], 0 offen sc1\n"
    "s_add_u32 s43, s51, 1\n"
    "s_lshr_b32 s43, s43, 1\n"
    "s_mul_i32 s43, s43, 3\n"
    "s_lshr_b32 s55, 0x23444688, s43\n"
    "s_mul_i32 s76, s53, 0x500\n"
    "s_lshl_b32 s75, s41, 2\n"
    "s_bfe_u32 s43, s55, 0x30000\n"
    "s_lshl4_add_u32 s43, s43, s75\n"
    "s_add_u32 s44, s43, s54\n"
    "s_sub_u32 s44, s44, 8\n"
    "s_add_u32 m0, s59, 0x17c00\n"
    "s_nop 0\n"
    "buffer_load_dwordx4 v10, s[8:11], s58 offen lds\n"
    "s_add_u32 s58, s58, 0xffffc900\n"
    "s_max_i32 s44, s44, 0\n"
    "s_mul_i32 s44, s44, 0xa00\n"
    "s_add_u32 s44, s44, s76\n"
    "s_mul_i32 s67, s43, 0x500\n"
    "s_cmp_ge_u32 s43, 0x48\n"
    "s_cselect_b32 s62, 0x280000, s44\n"
    "s_cselect_b32 s67, 0x30000, s67\n"
    "v_add_u32 v1, 0, v9\n"
    "v_mul_u32_u24 v2, 52, v1\n"
    "v_lshrrev_b32 v2, 8, v2\n"
    "s_add_u32 m0, s59, 0x18000\n"
    "s_nop 0\n"
    "buffer_load_dwordx4 v10, s[8:11], s58 offen lds\n"
    "s_add_u32 s58, s58, 0x3c00\n"
    "v_add_u32 v3, s75, v2\n"
    "v_xor_b32 v3, v3, v8\n"
    "v_lshl_add_u32 v1, v1, 4, v3\n"
    "v_mul_u32_u24 v2, 0x50, v2\n"
    "v_add_lshl_u32 v12, v1, v2, 4\n"
    "v_add_u32 v1, 4, v9\n"
    "v_mul_u32_u24 v2, 52, v1\n"
    "v_lshrrev_b32 v2, 8, v2\n"
    "v_add_u32 v3, s75, v2\n"
    "v_xor_b32 v3, v3, v8\n"
    "s_add_u32 m0, s59, 0x18400\n"
    "s_nop 0\n"
    "buffer_load_dwordx4 v10, s[8:11], s58 offen lds\n"
    "v_lshl_add_u32 v1, v1, 4, v3\n"
    "v_mul_u32_u24 v2, 0x50, v2\n"
    "v_add_lshl_u32 v13, v1, v2, 4\n"
    "v_add_u32 v1, 8, v9\n"
    "v_mul_u32_u24 v2, 52, v1\n"
    "v_lshrrev_b32 v2, 8, v2\n"
    "v_add_u32 v3, s75, v2\n"
    "v_xor_b32_e64 v3, v3, v8\n"
    "v_lshl_add_u32 v1, v1, 4, v3\n"
    "v_mul_u32_u24 v2, 0x50, v2\n"
    "v_add_lshl_u32 v14, v1, v2, 4\n"
    "v_add_u32 v1, 12, v9\n"
    "v_mul_u32_u24 v2, 52, v1\n"
    "v_lshrrev_b32 v2, 8, v2\n"
    "v_add_u32 v3, s75, v2\n"
    "v_xor_b32_e64 v3, v3, v8\n"
    "v_lshl_add_u32 v1, v1, 4, v3\n"
    "v_mul_u32_u24 v2, 0x50, v2\n"
    "v_add_lshl_u32 v15, v1, v2, 4\n"
    "v_add_u32 v1, 16, v9\n"
    "v_mul_u32_u24 v2, 52, v1\n"
    "v_lshrrev_b32 v2, 8, v2\n"
    "v_add_u32 v3, s75, v2\n"
    "v_xor_b32_e64 v3, v3, v8\n"
    "v_lshl_add_u32 v1, v1, 4, v3\n"
    "v_mul_u32_u24 v2, 0x50, v2\n"
    "v_add_lshl_u32 v16, v1, v2, 4\n"
    "v_bfe_u32 v17, v6, 2, 2\n"
    "v_and_b32 v20, 3, v6\n"
    "s_lshl2_add_u32 s43, s51, s41\n"
    "s_mul_i32 s43, s43, 40\n"
    "s_mul_i32 s44, s51, 0xa0\n"
    "s_and_b32 s44, s44, -128\n"
    "s_sub_u32 s43, s43, s44\n"
    "v_and_b32 v3, 1, v9\n"
    "v_lshl_add_u32 v3, v3, 2, v17\n"
    "v_lshlrev_b32 v3, 5, v3\n"
    "s_add_u32 m0, s67, 0\n"
    "s_nop 0\n"
    "buffer_load_dwordx4 v12, s[4:7], s62 offen lds\n"
    "v_mul_u32_u24 v4, 0x1800, v9\n"
    "v_lshl_add_u32 v4, v17, 8, v4\n"
    "v_add_u32 v4, 0x17000, v4\n"
    "v_lshl_add_u32 v1, v20, 3, s43\n"
    "v_xor_b32 v1, v1, v3\n"
    "v_add_u32 v21, v1, v4\n"
    "s_add_u32 s43, s43, 32\n"
    "v_xor_b32 v1, s43, v3\n"
    "v_min_u32 v2, 2, v20\n"
    "v_lshl_add_u32 v1, v2, 11, v1\n"
    "s_add_u32 m0, s67, 0x400\n"
    "s_nop 0\n"
    "buffer_load_dwordx4 v13, s[4:7], s62 offen lds\n"
    "v_add_u32 v22, v1, v4\n"
    "s_mul_i32 s43, s41, 0xf00\n"
    "v_add_u32 v23, s43, v7\n"
    "s_add_u32 s43, s43, 0xc00\n"
    "v_mul_u32_u24 v1, 12, v9\n"
    "v_min_u32 v2, 11, v8\n"
    "v_add_u32 v1, v1, v2\n"
    "v_lshl_add_u32 v24, v1, 4, s43\n"
    "v_cmp_gt_u32 vcc, 12, v8\n"
    "v_mov_b32 v2, 0x80000000\n"
    "s_add_u32 m0, s67, 0x800\n"
    "s_nop 0\n"
    "buffer_load_dwordx4 v14, s[4:7], s62 offen lds\n"
    "s_mul_i32 s43, s51, 20\n"
    "s_add_u32 s43, s43, s57\n"
    "s_mul_i32 s77, s43, 0x3c00\n"
    "s_add_u32 s77, s77, 0x11fc000\n"
    "v_cndmask_b32 v24, v2, v24, vcc\n"
    "s_lshl_b32 s60, s43, 10\n"
    "s_add_u32 s60, s60, 0x50000\n"
    "s_cmp_ge_u32 s57, 20\n"
    "s_cselect_b32 s60, 0x1454000, s60\n"
    "s_mul_i32 s43, s51, 0x5000\n"
    "s_add_u32 m0, s67, 0xc00\n"
    "s_nop 0\n"
    "buffer_load_dwordx4 v15, s[4:7], s62 offen lds\n"
    "s_add_u32 s61, s61, s43\n"
    "s_add_u32 s61, s61, 0x50000\n"
    "s_add_u32 m0, s67, 0x1000\n"
    "s_nop 0\n"
    "buffer_load_dwordx4 v16, s[4:7], s62 offen lds\n"
    "s_mov_b32 s57, 0x3f803f80\n"
    "s_mov_b32 s58, 0xffff0000\n"
    "s_bfe_u32 s43, s55, 0x30003\n"
    "s_lshl4_add_u32 s43, s43, s75\n"
    "s_add_u32 s44, s43, s54\n"
    "s_sub_u32 s44, s44, 8\n"
    "s_max_i32 s44, s44, 0\n"
    "s_mul_i32 s44, s44, 0xa00\n"
    "s_add_u32 s44, s44, s76\n"
    "s_mul_i32 s68, s43, 0x500\n"
    "s_cmp_ge_u32 s43, 0x48\n"
    "s_cselect_b32 s63, 0x280000, s44\n"
    "s_cselect_b32 s68, 0x30000, s68\n"
    "s_bfe_u32 s43, s55, 0x30006\n"
    "s_lshl4_add_u32 s43, s43, s75\n"
    "s_add_u32 s44, s43, s54\n"
    "s_sub_u32 s44, s44, 8\n"
    "s_max_i32 s44, s44, 0\n"
    "s_mul_i32 s44, s44, 0xa00\n"
    "s_add_u32 s44, s44, s76\n"
    "s_mul_i32 s69, s43, 0x500\n"
    "s_cmp_ge_u32 s43, 0x48\n"
    "s_cselect_b32 s64, 0x280000, s44\n"
    "s_cselect_b32 s69, 0x30000, s69\n"
    "s_bfe_u32 s43, s55, 0x30009\n"
    "s_lshl4_add_u32 s43, s43, s75\n"
    "s_add_u32 s44, s43, s54\n"
    "s_sub_u32 s44, s44, 8\n"
    "s_max_i32 s44, s44, 0\n"
    "s_mul_i32 s44, s44, 0xa00\n"
    "s_add_u32 s44, s44, s76\n"
    "s_mul_i32 s70, s43, 0x500\n"
    "s_cmp_ge_u32 s43, 0x48\n"
    "s_cselect_b32 s65, 0x280000, s44\n"
    "s_cselect_b32 s70, 0x30000, s70\n"
    "s_bfe_u32 s43, s55, 0x3000c\n"
    "s_lshl4_add_u32 s43, s43, s75\n"
    "s_add_u32 s44, s43, s54\n"
    "s_sub_u32 s44, s44, 8\n"
    "s_max_i32 s44, s44, 0\n"
    "s_mul_i32 s44, s44, 0xa00\n"
    "s_add_u32 s44, s44, s76\n"
    "s_mul_i32 s71, s43, 0x500\n"
    "s_cmp_ge_u32 s43, 0x48\n"
    "s_cselect_b32 s66, 0x280000, s44\n"
    "s_cselect_b32 s71, 0x30000, s71\n"
    "s_lshl4_add_u32 s56, s49, s50\n"
    "s_lshl_b32 s56, s56, 10\n"
    "s_add_u32 s56, s56, 0xc0000\n"
    "s_cmp_lt_u32 s41, 2\n"
    "s_cselect_b32 s43, s56, s60\n"
    "s_and_b32 s44, s41, 1\n"
    "s_lshl_b32 s44, s44, 9\n"
    "s_add_u32 s43, s43, s44\n"
    "s_cmp_eq_u32 s41, 3\n"
    "s_cselect_b32 s43, 0x1454000, s43\n"
    "v_mov_b32 v26, 0\n"
    "v_mov_b32 v27, 0\n"
    "v_lshl_add_u32 v28, v6, 3, s43\n"
    "buffer_store_dwordx2 v[26:27], v28, s[12:15], 0 offen sc1\n"
    "s_lshr_b32 s43, s40, 1\n"
    "s_mul_i32 s44, s43, 52\n"
    "s_lshr_b32 s44, s44, 8\n"
    "s_sub_u32 s45, s43, s44\n"
    "s_lshr_b32 s46, s41, 1\n"
    "s_lshl1_add_u32 s45, s45, s46\n"
    "s_mul_i32 s78, s45, 20\n"
    "s_and_b32 s46, s41, 1\n"
    "s_lshl3_add_u32 s78, s46, s78\n"
    "s_lshl2_add_u32 s45, s44, s41\n"
    "s_mul_i32 s45, s45, 40\n"
    "s_add_u32 s45, s45, 16\n"
    "s_lshl2_add_u32 s44, s44, s44\n"
    "s_add_u32 s44, s44, 4\n"
    "s_cmp_eq_u32 s43, s44\n"
    "s_cselect_b32 s78, s45, s78\n"
    "s_cselect_b32 s79, 16, 0\n"
    "s_sub_u32 s43, s43, 20\n"
    "s_lshl2_add_u32 s43, s43, s41\n"
    "s_lshl_b32 s43, s43, 3\n"
    "s_movk_i32 s80, 0x500\n"
    "s_movk_i32 s82, 0x80\n"
    "s_mul_i32 s81, s47, 0x280\n"
    "s_mov_b32 s22, 0xc8000\n"
    "s_cmp_ge_u32 s40, 40\n"
    "s_cselect_b32 s78, s43, s78\n"
    "s_cselect_b32 s79, 0, s79\n"
    "s_cselect_b32 s22, 0x21c000, s22\n"
    "s_and_b32 s43, s40, 3\n"
    "s_mul_i32 s43, s43, 0xa0\n"
    "s_lshl3_add_u32 s43, s41, s43\n"
    "s_sub_u32 s44, s40, 0x94\n"
    "s_lshr_b32 s44, s44, 2\n"
    "s_lshl_b32 s44, s44, 7\n"
    "s_cmp_ge_u32 s40, 0x94\n"
    "s_cselect_b32 s78, s43, s78\n"
    "s_cselect_b32 s81, s44, s81\n"
    "s_cselect_b32 s80, 0xd80, s80\n"
    "s_cselect_b32 s82, 0x1b000, s82\n"
    "v_lshrrev_b32 v1, 3, v6\n"
    "v_and_b32 v2, 7, v6\n"
    "v_lshrrev_b32 v3, 5, v6\n"
    "v_mad_u32_u24 v1, v3, s79, v1\n"
    "v_add_u32_e64 v1, s78, v1\n"
    "v_lshl_add_u32 v2, v2, 4, s81\n"
    "v_mad_u32_u24 v11, v1, s80, v2\n"
    "s_mov_b32 s90, 0\n"
    "s_waitcnt vmcnt(6)\n"
    "s_barrier\n"
    "ds_read_b64_tr_b16 v[128:129], v21\n"
    "ds_read_b64_tr_b16 v[130:131], v21 offset:1024\n"
    "ds_read_b64_tr_b16 v[132:133], v21 offset:2048\n"
    "ds_read_b64_tr_b16 v[134:135], v21 offset:3072\n"
    "ds_read_b64_tr_b16 v[136:137], v21 offset:4096\n"
    "ds_read_b64_tr_b16 v[138:139], v21 offset:5120\n"
    "ds_read_b64_tr_b16 v[140:141], v22\n"
    "ds_read_b64_tr_b16 v[142:143], v22 offset:1024\n"
    "s_waitcnt lgkmcnt(6)\n"
    "buffer_store_dwordx4 v[128:131], v23, s[12:15], s77 offen sc1\n"
    "s_waitcnt lgkmcnt(4)\n"
    "buffer_store_dwordx4 v[132:135], v23, s[12:15], s77 offen offset:1024 sc1\n"
    "s_waitcnt lgkmcnt(2)\n"
    "buffer_store_dwordx4 v[136:139], v23, s[12:15], s77 offen offset:2048 sc1\n"
    "s_waitcnt lgkmcnt(0)\n"
    "buffer_store_dwordx4 v[140:143], v24, s[12:15], s77 offen sc1\n"
    "v_readfirstlane_b32 s38, v18\n"
    "v_readfirstlane_b32 s39, v19\n"
    "s_add_u32 s38, s38, 0x7f4a7c15\n"
    "s_addc_u32 s39, s39, 0x9e3779b9\n"
    "s_or_b32 s38, s38, 1\n"
    "v_mov_b32 v18, s38\n"
    "v_mov_b32 v19, s39\n"
    "s_add_u32 m0, s68, 0\n"
    "s_nop 0\n"
    "buffer_load_dwordx4 v12, s[4:7], s63 offen lds\n"
    "v_cmp_gt_u32 vcc, 16, v0\n"
    "v_mov_b32 v4, 0x80000000\n"
    "v_lshlrev_b32 v2, 3, v0\n"
    "v_lshlrev_b32 v1, 10, v0\n"
    "v_cndmask_b32 v29, v4, v2, vcc\n"
    "v_cmp_gt_u32 s[28:29], 20, v0\n"
    "v_cmp_gt_u32 vcc, 64, v0\n"
    "s_nop 0\n"
    "v_cndmask_b32 v30, v4, v1, s[28:29]\n"
    "v_cndmask_b32 v31, v4, v2, vcc\n"
    "s_add_u32 m0, s68, 0x400\n"
    "s_nop 0\n"
    "buffer_load_dwordx4 v13, s[4:7], s63 offen lds\n"
    "s_nop 9\n"
    "s_add_u32 m0, s68, 0x800\n"
    "s_nop 0\n"
    "buffer_load_dwordx4 v14, s[4:7], s63 offen lds\n"
    "s_nop 9\n"
    "s_add_u32 m0, s68, 0xc00\n"
    "s_nop 0\n"
    "buffer_load_dwordx4 v15, s[4:7], s63 offen lds\n"
    "s_nop 9\n"
    "s_add_u32 m0, s68, 0x1000\n"
    "s_nop 0\n"
    "buffer_load_dwordx4 v16, s[4:7], s63 offen lds\n"
    "s_nop 9\n"
    "s_add_u32 m0, s69, 0\n"
    "s_nop 0\n"
    "buffer_load_dwordx4 v12, s[4:7], s64 offen lds\n"
    "s_nop 9\n"
    "s_add_u32 m0, s48, 0x1d000\n"
    "s_nop 0\n"
    "buffer_load_dwordx4 v11, s[20:23], s90 offen lds\n"
    "s_add_u32 s90, s90, s82\n"
    "s_waitcnt vmcnt(7)\n"
    "s_barrier\n"
    "s_cmp_lt_u32 s41, 1\n"
    "s_cbranch_scc0 .Lskip_3_%=\n"
    "buffer_store_dwordx2 v[18:19], v31, s[12:15], s60 offen sc1\n"
    ".Lskip_3_%=:\n"
    "s_add_u32 m0, s48, 0x1e000\n"
    "s_nop 0\n"
    "buffer_load_dwordx4 v11, s[20:23], s90 offen lds\n"
    "s_add_u32 s90, s90, s82\n"
    "s_add_u32 m0, s48, 0x1f000\n"
    "s_nop 0\n"
    "buffer_load_dwordx4 v11, s[20:23], s90 offen lds\n"
    "s_add_u32 s90, s90, s82\n"
    "v_mov_b32 v36, s57\n"
    "v_mov_b32 v37, s57\n"
    "s_add_u32 m0, s48, 0x20000\n"
    "s_nop 0\n"
    "buffer_load_dwordx4 v11, s[20:23], s90 offen lds\n"
    "s_add_u32 s90, s90, s82\n"
    "v_mov_b32 v38, s57\n"
    "v_mov_b32 v39, s57\n"
    "v_cmp_eq_u32 vcc, 0, v8\n"
    "s_nop 1\n"
    "s_nop 0\n"
    "v_cndmask_b32_e64 v1, 0, v36, vcc\n"
    "v_accvgpr_write_b32 a0, v1\n"
    "v_accvgpr_write_b32 a1, v1\n"
    "v_accvgpr_write_b32 a2, v1\n"
    "v_accvgpr_write_b32 a3, v1\n"
    "v_min_u32 v1, 0x4f, v0\n"
    "s_add_u32 m0, s48, 0x21000\n"
    "s_nop 0\n"
    "buffer_load_dwordx4 v11, s[20:23], s90 offen lds\n"
    "s_add_u32 s90, s90, s82\n"
    "v_lshlrev_b32_e64 v1, 4, v1\n"
    "v_add_u32 v1, 0x16800, v1\n"
    "ds_write_b128 v1, v[36:39]\n"
    "v_bfe_i32 v2, v6, 2, 1\n"
    "v_lshlrev_b32 v1, 2, v8\n"
    "v_and_b32 v2, 1.0, v2\n"
    "v_add_u32 v1, 0x16d00, v1\n"
    "ds_write_b32 v1, v2\n"
    "s_add_u32 m0, s69, 0x400\n"
    "s_nop 0\n"
    "buffer_load_dwordx4 v13, s[4:7], s64 offen lds\n"
    "s_add_u32 m0, s69, 0x800\n"
    "s_nop 0\n"
    "buffer_load_dwordx4 v14, s[4:7], s64 offen lds\n"
    "s_cmp_ge_u32 s40, 0x94\n"
    "s_cbranch_scc1 .Lpw1_4_%=\n"
    "s_mov_b32 s83, 0x1454000\n"
    "s_mov_b32 s45, 0x19000\n"
    "s_mov_b32 s46, 0x100000\n"
    "s_cmp_ge_u32 s40, 40\n"
    "s_cselect_b32 s45, 0x43800, s45\n"
    "s_cselect_b32 s46, 0x1af000, s46\n"
    "s_lshr_b32 s43, s40, 1\n"
    "s_mul_i32 s43, s43, 0x1400\n"
    "s_add_u32 s46, s46, s43\n"
    "s_lshl_b32 s43, s47, 2\n"
    "s_mul_i32 s43, s43, s45\n"
    "s_add_u32 s46, s46, s43\n"
    "s_lshl_b32 s44, s41, 3\n"
    "s_lshr_b32 s43, 0xc080400, s44\n"
    "s_and_b32 s84, s43, 3\n"
    "s_mul_i32 s84, s84, s45\n"
    "s_add_u32 s84, s84, s46\n"
    "s_bfe_u32 s43, s43, 0x30002\n"
    "s_lshl_b32 s43, s43, 10\n"
    "s_add_u32 s84, s84, s43\n"
    "s_lshr_b32 s43, 0x5011110, s44\n"
    "s_and_b32 s85, s43, 3\n"
    "s_mul_i32 s85, s85, s45\n"
    "s_add_u32 s85, s85, s46\n"
    "s_bfe_u32 s43, s43, 0x30002\n"
    "s_lshl_b32 s43, s43, 10\n"
    "s_add_u32 s85, s85, s43\n"
    "s_lshr_b32 s43, 0x6020d09, s44\n"
    "s_and_b32 s86, s43, 3\n"
    "s_mul_i32 s86, s86, s45\n"
    "s_add_u32 s86, s86, s46\n"
    "s_bfe_u32 s43, s43, 0x30002\n"
    "s_lshl_b32 s43, s43, 10\n"
    "s_add_u32 s86, s86, s43\n"
    "s_lshr_b32 s43, 0x13120e0a, s44\n"
    "s_and_b32 s87, s43, 3\n"
    "s_mul_i32 s87, s87, s45\n"
    "s_add_u32 s87, s87, s46\n"
    "s_bfe_u32 s43, s43, 0x30002\n"
    "s_lshl_b32 s43, s43, 10\n"
    "s_add_u32 s87, s87, s43\n"
    "s_lshr_b32 s43, 0xf0b0703, s44\n"
    "s_and_b32 s88, s43, 3\n"
    "s_mul_i32 s88, s88, s45\n"
    "s_add_u32 s88, s88, s46\n"
    "s_bfe_u32 s43, s43, 0x30002\n"
    "s_lshl_b32 s43, s43, 10\n"
    "s_add_u32 s88, s88, s43\n"
    "s_lshr_b32 s89, 0x8080202, s44\n"
    "s_branch .Lpj_5_%=\n"
    ".Lpw1_4_%=:\n"
    "s_and_b32 s43, s40, 3\n"
    "s_lshr_b32 s44, s81, 6\n"
    "s_lshr_b32 s45, s41, 1\n"
    "s_add_u32 s44, s44, s45\n"
    "s_and_b32 s84, s41, 1\n"
    "s_mul_i32 s85, s44, 0x4f\n"
    "s_lshr_b32 s85, s85, 10\n"
    "s_mul_i32 s86, s85, 13\n"
    "s_sub_u32 s86, s44, s86\n"
    "s_lshr_b32 s87, s86, 1\n"
    "s_and_b32 s88, s86, 1\n"
    "s_lshl1_add_u32 s88, s88, s84\n"
    "s_sub_u32 s45, s86, 9\n"
    "s_add_u32 s46, s84, 5\n"
    "s_cmp_gt_u32 s86, 8\n"
    "s_cselect_b32 s87, s45, s87\n"
    "s_cselect_b32 s88, s46, s88\n"
    "s_cmp_eq_u32 s86, 8\n"
    "s_cselect_b32 s87, s84, s87\n"
    "s_cselect_b32 s88, 4, s88\n"
    "s_lshl2_add_u32 s87, s85, s87\n"
    "s_lshl1_add_u32 s45, s86, s84\n"
    "s_lshl2_add_u32 s45, s45, 2\n"
    "s_cmp_ge_u32 s44, 52\n"
    "s_cselect_b32 s87, s45, s87\n"
    "s_cselect_b32 s88, 4, s88\n"
    "s_cmp_eq_u32 s88, 4\n"
    "s_cselect_b32 s89, 31, 0\n"
    "s_lshl3_add_u32 s83, s87, s88\n"
    "s_lshl2_add_u32 s83, s83, s43\n"
    "s_lshl_b32 s83, s83, 6\n"
    "s_add_u32 s83, s83, 0x614000\n"
    "s_mul_i32 s46, s87, 0x23000\n"
    "s_add_u32 s46, s46, 0x3e4000\n"
    "s_lshl_b32 s45, s88, 10\n"
    "s_add_u32 s46, s46, s45\n"
    "s_cmp_ge_u32 s88, 5\n"
    "s_cselect_b32 s45, 0x17c00, 0\n"
    "s_movk_i32 s44, 0x1400\n"
    "s_cselect_b32 s44, 0x800, s44\n"
    "s_add_u32 s46, s46, s45\n"
    "s_mul_i32 s43, s43, 5\n"
    "s_mul_i32 s43, s43, s44\n"
    "s_add_u32 s84, s46, s43\n"
    "s_add_u32 s85, s84, s44\n"
    "s_add_u32 s86, s85, s44\n"
    "s_add_u32 s87, s86, s44\n"
    "s_add_u32 s88, s87, s44\n"
    ".Lpj_5_%=:\n"
    "v_lshl_add_u32 v1, v9, 3, v17\n"
    "v_lshlrev_b32 v1, 7, v1\n"
    "v_add_u32 v1, 0x1d000, v1\n"
    "s_lshr_b32 s43, s41, 1\n"
    "s_add_u32 m0, s69, 0xc00\n"
    "s_nop 0\n"
    "buffer_load_dwordx4 v15, s[4:7], s64 offen lds\n"
    "s_lshl_b32 s43, s43, 3\n"
    "s_and_b32 s44, s41, 1\n"
    "s_add_u32 s43, s43, s44\n"
    "s_lshl_b32 s43, s43, 3\n"
    "v_lshl_add_u32 v2, v20, 4, v1\n"
    "v_add_u32 v2, s43, v2\n"
    "s_lshl_b32 s43, s41, 5\n"
    "v_lshl_add_u32 v3, v20, 3, v1\n"
    "v_add_u32 v3, s43, v3\n"
    "s_bitcmp1_b32 s89, 0\n"
    "s_cselect_b64 vcc, -1, 0\n"
    "v_cndmask_b32 v34, v2, v3, vcc\n"
    "s_add_u32 m0, s69, 0x1000\n"
    "s_nop 0\n"
    "buffer_load_dwordx4 v16, s[4:7], s64 offen lds\n"
    "s_bitcmp1_b32 s89, 1\n"
    "s_cselect_b64 vcc, -1, 0\n"
    "v_cndmask_b32 v35, v2, v3, vcc\n"
    "s_bitcmp1_b32 s89, 2\n"
    "s_cselect_b64 vcc, -1, 0\n"
    "v_cndmask_b32 v40, v2, v3, vcc\n"
    "s_bitcmp1_b32 s89, 3\n"
    "s_cselect_b64 vcc, -1, 0\n"
    "v_cndmask_b32 v41, v2, v3, vcc\n"
    "s_add_u32 m0, s70, 0\n"
    "s_nop 0\n"
    "buffer_load_dwordx4 v12, s[4:7], s65 offen lds\n"
    "s_bitcmp1_b32 s89, 4\n"
    "s_cselect_b64 vcc, -1, 0\n"
    "v_cndmask_b32 v42, v2, v3, vcc\n"
    "v_cmp_eq_u32 vcc, 0, v8\n"
    "v_mov_b32 v4, 0x80000000\n"
    "s_nop 0\n"
    "v_cndmask_b32 v43, v4, v6, vcc\n"
    "v_add_u32 v1, 8, v8\n"
    "v_mul_u32_u24 v1, 0x500, v1\n"
    "s_add_u32 m0, s70, 0x400\n"
    "s_nop 0\n"
    "buffer_load_dwordx4 v13, s[4:7], s65 offen lds\n"
    "v_and_b32 v3, 11, v8\n"
    "v_cmp_eq_u32 vcc, 0, v3\n"
    "v_subrev_u32 v2, 8, v8\n"
    "v_min_u32 v2, 0x100, v2\n"
    "v_mov_b32 v4, 0x48\n"
    "v_cndmask_b32 v2, v2, v4, vcc\n"
    "v_mul_u32_u24 v2, 0x500, v2\n"
    "v_xor_b32 v3, 8, v8\n"
    "v_xor_b32 v3, v3, v9\n"
    "v_xor_b32_e64 v4, 0, v3\n"
    "s_add_u32 m0, s70, 0x800\n"
    "s_nop 0\n"
    "buffer_load_dwordx4 v14, s[4:7], s65 offen lds\n"
    "v_lshl_add_u32 v44, v4, 4, v1\n"
    "v_lshl_add_u32 v45, v4, 4, v2\n"
    "v_xor_b32 v4, 4, v3\n"
    "v_lshl_add_u32 v46, v4, 4, v1\n"
    "v_lshl_add_u32 v47, v4, 4, v2\n"
    "v_xor_b32 v4, 8, v3\n"
    "v_lshl_add_u32 v48, v4, 4, v1\n"
    "v_lshl_add_u32 v49, v4, 4, v2\n"
    "v_xor_b32 v4, 12, v3\n"
    "v_lshl_add_u32 v50, v4, 4, v1\n"
    "s_add_u32 m0, s70, 0xc00\n"
    "s_nop 0\n"
    "buffer_load_dwordx4 v15, s[4:7], s65 offen lds\n"
    "v_lshl_add_u32 v51, v4, 4, v2\n"
    "s_mul_i32 s59, s51, 0x4b000\n"
    "s_add_u32 s59, s59, 0x11fc000\n"
    "v_mul_u32_u24 v1, 0x500, v9\n"
    "v_lshl_add_u32 v1, v8, 4, v1\n"
    "v_add_u32_e64 v1, s67, v1\n"
    "ds_read_b128 v[52:55], v1\n"
    "ds_read_b128 v[56:59], v1 offset:256\n"
    "ds_read_b128 v[60:63], v1 offset:512\n"
    "ds_read_b128 v[64:67], v1 offset:768\n"
    "ds_read_b128 v[68:71], v1 offset:1024\n"
    "s_lshr_b32 s43, s54, 2\n"
    "s_lshl1_add_u32 s43, s51, s43\n"
    "s_and_b32 s45, s41, 1\n"
    "s_add_u32 s43, s43, s45\n"
    "s_add_u32 m0, s70, 0x1000\n"
    "s_nop 0\n"
    "buffer_load_dwordx4 v16, s[4:7], s65 offen lds\n"
    "s_lshl_b32 s45, s53, 8\n"
    "s_add_u32 s43, s43, s45\n"
    "s_lshl_b32 s43, s43, 6\n"
    "s_add_u32 s43, s43, 0x8000\n"
    "s_lshr_b32 s45, s41, 1\n"
    "s_xor_b32 s45, s45, s51\n"
    "s_bitcmp1_b32 s45, 0\n"
    "s_cselect_b32 s74, s43, 0x1454000\n"
    "s_waitcnt lgkmcnt(4)\n"
    "v_dot2_f32_bf16 v92, v52, s57, 0\n"
    "v_dot2_f32_bf16 v93, v52, v52, 0\n"
    "s_cmp_lt_u32 s41, 1\n"
    "s_cbranch_scc0 .Lskip_6_%=\n"
    "buffer_load_dwordx2 v[32:33], v30, s[12:15], s61 offen sc1\n"
    ".Lskip_6_%=:\n"
    "v_dot2_f32_bf16 v92, v53, s57, v92\n"
    "v_dot2_f32_bf16 v93, v53, v53, v93\n"
    "v_dot2_f32_bf16 v92, v54, s57, v92\n"
    "v_dot2_f32_bf16 v93, v54, v54, v93\n"
    "v_dot2_f32_bf16 v92, v55, s57, v92\n"
    "v_dot2_f32_bf16 v93, v55, v55, v93\n"
    "s_waitcnt lgkmcnt(3)\n"
    "v_dot2_f32_bf16 v92, v56, s57, v92\n"
    "v_dot2_f32_bf16 v93, v56, v56, v93\n"
    "v_dot2_f32_bf16 v92, v57, s57, v92\n"
    "v_dot2_f32_bf16 v93, v57, v57, v93\n"
    "s_add_u32 m0, s71, 0\n"
    "s_nop 0\n"
    "buffer_load_dwordx4 v12, s[4:7], s66 offen lds\n"
    "v_dot2_f32_bf16 v92, v58, s57, v92\n"
    "v_dot2_f32_bf16 v93, v58, v58, v93\n"
    "v_dot2_f32_bf16 v92, v59, s57, v92\n"
    "v_dot2_f32_bf16 v93, v59, v59, v93\n"
    "s_waitcnt lgkmcnt(2)\n"
    "v_dot2_f32_bf16 v92, v60, s57, v92\n"
    "v_dot2_f32_bf16 v93, v60, v60, v93\n"
    "v_dot2_f32_bf16 v92, v61, s57, v92\n"
    "v_dot2_f32_bf16 v93, v61, v61, v93\n"
    "v_dot2_f32_bf16 v92, v62, s57, v92\n"
    "v_dot2_f32_bf16 v93, v62, v62, v93\n"
    "s_add_u32 m0, s71, 0x400\n"
    "s_nop 0\n"
    "buffer_load_dwordx4 v13, s[4:7], s66 offen lds\n"
    "v_dot2_f32_bf16 v92, v63, s57, v92\n"
    "v_dot2_f32_bf16 v93, v63, v63, v93\n"
    "s_waitcnt lgkmcnt(1)\n"
    "v_dot2_f32_bf16 v92, v64, s57, v92\n"
    "v_dot2_f32_bf16 v93, v64, v64, v93\n"
    "v_dot2_f32_bf16 v92, v65, s57, v92\n"
    "v_dot2_f32_bf16 v93, v65, v65, v93\n"
    "v_dot2_f32_bf16 v92, v66, s57, v92\n"
    "v_dot2_f32_bf16 v93, v66, v66, v93\n"
    "v_dot2_f32_bf16 v92, v67, s57, v92\n"
    "v_dot2_f32_bf16 v93, v67, v67, v93\n"
    "s_add_u32 m0, s71, 0x800\n"
    "s_nop 0\n"
    "buffer_load_dwordx4 v14, s[4:7], s66 offen lds\n"
    "s_waitcnt lgkmcnt(0)\n"
    "v_dot2_f32_bf16 v92, v68, s57, v92\n"
    "v_dot2_f32_bf16 v93, v68, v68, v93\n"
    "v_dot2_f32_bf16 v92, v69, s57, v92\n"
    "v_dot2_f32_bf16 v93, v69, v69, v93\n"
    "v_dot2_f32_bf16 v92, v70, s57, v92\n"
    "v_dot2_f32_bf16 v93, v70, v70, v93\n"
    "v_dot2_f32_bf16 v92, v71, s57, v92\n"
    "v_dot2_f32_bf16 v93, v71, v71, v93\n"
    "v_lshlrev_b32 v1, 2, v9\n"
    "v_lshl_add_u32 v1, v8, 4, v1\n"
    "s_add_u32 m0, s71, 0xc00\n"
    "s_nop 0\n"
    "buffer_load_dwordx4 v15, s[4:7], s66 offen lds\n"
    "v_lshrrev_b32_e64 v2, 2, v8\n"
    "v_add_f32_dpp v92, v92, v92 row_ror:8 row_mask:0xf bank_mask:0xf\n"
    "v_add_f32_dpp v93, v93, v93 row_ror:8 row_mask:0xf bank_mask:0xf\n"
    "v_lshl_or_b32 v95, v2, 28, v1\n"
    "v_add_f32_dpp v92, v92, v92 row_ror:4 row_mask:0xf bank_mask:0xf\n"
    "v_add_f32_dpp v93, v93, v93 row_ror:4 row_mask:0xf bank_mask:0xf\n"
    "v_and_b32 v1, 1, v8\n"
    "v_add_f32_dpp v92, v92, v92 row_ror:2 row_mask:0xf bank_mask:0xf\n"
    "v_add_f32_dpp v93, v93, v93 row_ror:2 row_mask:0xf bank_mask:0xf\n"
    "v_cmp_eq_u32_e64 vcc, 1, v1\n"
    "s_add_u32 m0, s71, 0x1000\n"
    "s_nop 0\n"
    "buffer_load_dwordx4 v16, s[4:7], s66 offen lds\n"
    "v_add_f32_dpp v92, v92, v92 row_ror:1 row_mask:0xf bank_mask:0xf\n"
    "v_add_f32_dpp v93, v93, v93 row_ror:1 row_mask:0xf bank_mask:0xf\n"
    "v_mul_f32 v92, 0xbacccccd, v92\n"
    "v_mul_f32 v93, 0x3acccccd, v93\n"
    "v_fma_f32 v93, -v92, v92, v93\n"
    "v_max_f32 v93, 0, v93\n"
    "v_add_f32 v93, 0x3727c5ac, v93\n"
    "v_rsq_f32 v93, v93\n"
    "s_nop 0\n"
    "v_cndmask_b32_e64 v94, v92, v93, vcc\n"
    "s_waitcnt vmcnt(5)\n"
    "v_cmp_ne_u64 s[34:35], s[38:39], v[32:33]\n"
    "s_nop 0\n"
    "s_and_b64 s[34:35], s[34:35], s[28:29]\n"
    "s_cbranch_scc0 .Lgok_7_%=\n"
    "s_movk_i32 s46, 0x4e20\n"
    ".Lgspin_8_%=:\n"
    "s_sleep 0\n"
    "buffer_load_dwordx2 v[32:33], v30, s[12:15], s61 offen sc1\n"
    "s_sub_u32 s46, s46, 1\n"
    "s_cbranch_scc1 .Lgok_7_%=\n"
    "s_waitcnt vmcnt(0)\n"
    "v_cmp_ne_u64 s[34:35], s[38:39], v[32:33]\n"
    "s_nop 0\n"
    "s_and_b64 s[34:35], s[34:35], s[28:29]\n"
    "s_cbranch_scc1 .Lgspin_8_%=\n"
    ".Lgok_7_%=:\n"
    "s_barrier\n"
    "buffer_load_dwordx4 a[40:43], v24, s[12:15], s59 offen sc1\n"
    "buffer_load_dwordx4 a[32:35], v23, s[12:15], s59 offen offset:1024 sc1\n"
    "buffer_load_dwordx4 a[28:31], v23, s[12:15], s59 offen sc1\n"
    "buffer_load_dwordx4 a[36:39], v23, s[12:15], s59 offen offset:2048 sc1\n"
    "s_add_u32 s59, s59, 0x3c00\n"
    "buffer_load_dwordx4 a[56:59], v24, s[12:15], s59 offen sc1\n"
    "buffer_load_dwordx4 a[48:51], v23, s[12:15], s59 offen offset:1024 sc1\n"
    "s_lshl_b32 s89, s40, 10\n"
    "buffer_load_dwordx4 a[44:47], v23, s[12:15], s59 offen sc1\n"
    "s_add_u32 s89, s89, 0x80000\n"
    "s_waitcnt vmcnt(7)\n"
    "s_barrier\n"
    "ds_read_b128 a[156:159], v45\n"
    "ds_read_b128 a[160:163], v47\n"
    "ds_read_b128 a[164:167], v44\n"
    "ds_read_b128 a[168:171], v46\n"
    "ds_read_b128 a[172:175], v44 offset:20480\n"
    "ds_read_b128 a[176:179], v46 offset:20480\n"
    "buffer_load_dwordx4 a[52:55], v23, s[12:15], s59 offen offset:2048 sc1\n"
    "s_add_u32 s59, s59, 0x3c00\n"
    "buffer_load_dwordx4 a[72:75], v24, s[12:15], s59 offen sc1\n"
    "buffer_load_dwordx4 a[64:67], v23, s[12:15], s59 offen offset:1024 sc1\n"
    "buffer_load_dwordx4 a[60:63], v23, s[12:15], s59 offen sc1\n"
    "buffer_load_dwordx4 a[68:71], v23, s[12:15], s59 offen offset:2048 sc1\n"
    "s_add_u32 s59, s59, 0x3c00\n"
    "buffer_load_dwordx4 a[88:91], v24, s[12:15], s59 offen sc1\n"
    "buffer_load_dwordx4 a[80:83], v23, s[12:15], s59 offen offset:1024 sc1\n"
    "buffer_load_dwordx4 a[76:79], v23, s[12:15], s59 offen sc1\n"
    "buffer_load_dwordx4 a[84:87], v23, s[12:15], s59 offen offset:2048 sc1\n"
    "s_add_u32 s59, s59, 0x3c00\n"
    "s_waitcnt vmcnt(15) lgkmcnt(5)\n"
    "v_mfma_f32_16x16x32_bf16 v[184:187], a[40:43], a[156:159], 0\n"
    "s_waitcnt vmcnt(11) lgkmcnt(4)\n"
    "ds_read_b64_tr_b16 a[4:5], v34\n"
    "ds_read_b64_tr_b16 a[6:7], v34 offset:512\n"
    "v_mfma_f32_16x16x32_bf16 v[184:187], a[56:59], a[160:163], v[184:187]\n"
    "buffer_load_dwordx4 a[104:107], v24, s[12:15], s59 offen sc1\n"
    "ds_read_b64_tr_b16 a[8:9], v35 offset:4096\n"
    "ds_read_b64_tr_b16 a[10:11], v35 offset:4608\n"
    "v_mfma_f32_16x16x32_bf16 v[144:147], a[32:35], a[156:159], 0\n"
    "s_waitcnt vmcnt(11)\n"
    "ds_read_b64_tr_b16 a[12:13], v40 offset:8192\n"
    "ds_read_b64_tr_b16 a[14:15], v40 offset:8704\n"
    "v_mfma_f32_16x16x32_bf16 v[144:147], a[48:51], a[160:163], v[144:147]\n"
    "ds_read_b128 a[180:183], v44 offset:40960\n"
    "v_mfma_f32_16x16x32_bf16 v[12:15], a[28:31], a[0:3], 0\n"
    "s_waitcnt vmcnt(10)\n"
    "ds_read_b64_tr_b16 a[16:17], v41 offset:12288\n"
    "ds_read_b64_tr_b16 a[18:19], v41 offset:12800\n"
    "v_mfma_f32_16x16x32_bf16 v[12:15], a[44:47], a[0:3], v[12:15]\n"
    "ds_read_b64_tr_b16 a[20:21], v42 offset:16384\n"
    "ds_read_b64_tr_b16 a[22:23], v42 offset:16896\n"
    "s_mul_i32 s62, s51, 0x19000\n"
    "v_mfma_f32_16x16x32_bf16 v[164:167], a[156:159], a[36:39], 0\n"
    "buffer_load_dwordx4 a[96:99], v23, s[12:15], s59 offen offset:1024 sc1\n"
    "s_waitcnt vmcnt(10)\n"
    "s_add_u32 s62, s62, 0xf1000\n"
    "v_mfma_f32_16x16x32_bf16 v[164:167], a[160:163], a[52:55], v[164:167]\n"
    "ds_read_b128 a[184:187], v46 offset:40960\n"
    "s_waitcnt lgkmcnt(15)\n"
    "s_mul_i32 s78, s51, 0x43800\n"
    "v_mfma_f32_16x16x32_bf16 v[188:191], a[40:43], a[164:167], 0\n"
    "s_waitcnt lgkmcnt(14)\n"
    "s_add_u32 s78, s78, 0x1c8000\n"
    "s_lshr_b32 s60, s41, 1\n"
    "v_mfma_f32_16x16x32_bf16 v[188:191], a[56:59], a[168:171], v[188:191]\n"
    "s_add_u32 s60, s60, 52\n"
    "s_mul_i32 s79, s60, 0x1400\n"
    "s_add_u32 s79, s79, s78\n"
    "v_mfma_f32_16x16x32_bf16 v[148:151], a[32:35], a[164:167], 0\n"
    "s_mul_i32 s60, s41, 0x10400\n"
    "s_add_u32 s78, s78, s60\n"
    "v_lshrrev_b32 v102, 5, v6\n"
    "v_mfma_f32_16x16x32_bf16 v[148:151], a[48:51], a[168:171], v[148:151]\n"
    "buffer_load_dwordx4 a[92:95], v23, s[12:15], s59 offen sc1\n"
    "ds_read_b128 a[188:191], v44 offset:61440\n"
    "v_xor_b32 v102, s41, v102\n"
    "v_mfma_f32_16x16x32_bf16 v[128:131], a[28:31], a[164:167], 0\n"
    "v_lshl_or_b32 v10, v102, 31, v7\n"
    "v_lshlrev_b32 v102, 6, v20\n"
    "v_mfma_f32_16x16x32_bf16 v[128:131], a[44:47], a[168:171], v[128:131]\n"
    "v_lshl_add_u32 v102, v9, 4, v102\n"
    "v_add_u32 v16, 0x23000, v102\n"
    "v_mfma_f32_16x16x32_bf16 v[168:171], a[164:167], a[36:39], 0\n"
    "s_lshl_b32 s80, s50, 11\n"
    "s_add_u32 s80, s80, 0x5f1000\n"
    "s_and_b32 s60, s50, 3\n"
    "v_mfma_f32_16x16x32_bf16 v[168:171], a[168:171], a[52:55], v[168:171]\n"
    "ds_read_b128 a[192:195], v46 offset:61440\n"
    "s_waitcnt lgkmcnt(15)\n"
    "v_mfma_f32_16x16x32_bf16 v[192:195], a[40:43], a[172:175], 0\n"
    "buffer_load_dwordx4 a[100:103], v23, s[12:15], s59 offen offset:2048 sc1\n"
    "s_add_u32 s59, s59, 0x3c00\n"
    "s_waitcnt lgkmcnt(14)\n"
    "v_mfma_f32_16x16x32_bf16 v[192:195], a[56:59], a[176:179], v[192:195]\n"
    "s_cmp_eq_u32 s60, 3\n"
    "s_cselect_b32 s60, 0x80000000, 0\n"
    "v_or_b32 v21, s60, v7\n"
    "v_mfma_f32_16x16x32_bf16 v[152:155], a[32:35], a[172:175], 0\n"
    "v_lshrrev_b32 v102, 4, v0\n"
    "v_cmp_eq_u32 vcc, 4, v102\n"
    "v_mfma_f32_16x16x32_bf16 v[152:155], a[48:51], a[176:179], v[152:155]\n"
    "ds_read_b128 a[156:159], v49\n"
    "v_add_u32 v102, 1, v102\n"
    "v_lshlrev_b32 v102, 28, v102\n"
    "v_mfma_f32_16x16x32_bf16 v[132:135], a[28:31], a[172:175], 0\n"
    "v_cndmask_b32 v22, v7, v21, vcc\n"
    "v_and_b32 v102, 0x80000000, v102\n"
    "v_mfma_f32_16x16x32_bf16 v[132:135], a[44:47], a[176:179], v[132:135]\n"
    "buffer_load_dwordx4 a[120:123], v24, s[12:15], s59 offen sc1\n"
    "v_or_b32 v22, v22, v102\n"
    "v_cmp_gt_u32 vcc, 0x80, v0\n"
    "v_mfma_f32_16x16x32_bf16 v[172:175], a[172:175], a[36:39], 0\n"
    "buffer_load_dwordx4 a[112:115], v23, s[12:15], s59 offen offset:1024 sc1\n"
    "v_lshlrev_b32 v102, 3, v0\n"
    "v_mov_b32 v121, 0x80000000\n"
    "v_mfma_f32_16x16x32_bf16 v[172:175], a[176:179], a[52:55], v[172:175]\n"
    "ds_read_b128 a[160:163], v51\n"
    "s_waitcnt lgkmcnt(9)\n"
    "v_cndmask_b32 v31, v121, v102, vcc\n"
    "v_mfma_f32_16x16x32_bf16 v[196:199], a[40:43], a[180:183], 0\n"
    "buffer_load_dwordx4 a[108:111], v23, s[12:15], s59 offen sc1\n"
    "s_waitcnt lgkmcnt(4)\n"
    "s_and_b32 s61, s40, 7\n"
    "v_mfma_f32_16x16x32_bf16 v[196:199], a[56:59], a[184:187], v[196:199]\n"
    "s_lshl_b32 s61, s61, 7\n"
    "s_add_u32 s61, s61, 0x80000\n"
    "s_lshr_b32 s60, s50, 2\n"
    "v_mfma_f32_16x16x32_bf16 v[156:159], a[32:35], a[180:183], 0\n"
    "buffer_load_dwordx4 a[116:119], v23, s[12:15], s59 offen offset:2048 sc1\n"
    "s_add_u32 s59, s59, 0x3c00\n"
    "s_mul_i32 s60, s60, 13\n"
    "v_mfma_f32_16x16x32_bf16 v[156:159], a[48:51], a[184:187], v[156:159]\n"
    "ds_read_b128 a[164:167], v48\n"
    "s_and_b32 s76, s50, 3\n"
    "s_lshl1_add_u32 s75, s76, s60\n"
    "v_mfma_f32_16x16x32_bf16 v[136:139], a[28:31], a[180:183], 0\n"
    "buffer_store_dword v94, v95, s[12:15], s74 offen sc1\n"
    "v_add_u32 v102, s75, v17\n"
    "s_add_u32 s75, s60, s76\n"
    "v_mfma_f32_16x16x32_bf16 v[136:139], a[44:47], a[184:187], v[136:139]\n"
    "s_add_u32 s75, s75, 9\n"
    "v_cmp_eq_u32 vcc, 2, v17\n"
    "v_mov_b32 v103, s75\n"
    "v_mfma_f32_16x16x32_bf16 v[176:179], a[180:183], a[36:39], 0\n"
    "v_mfma_f32_16x16x32_bf16 a[24:27], a[4:7], v[36:39], 0\n"
    "buffer_store_dwordx4 a[4:7], v7, s[12:15], s84 offen sc1\n"
    "s_add_u32 s60, s60, 8\n"
    "v_mfma_f32_16x16x32_bf16 v[176:179], a[184:187], a[52:55], v[176:179]\n"
    "v_mfma_f32_16x16x32_bf16 a[24:27], a[8:11], v[36:39], a[24:27]\n"
    "buffer_store_dwordx4 a[8:11], v7, s[12:15], s85 offen sc1\n"
    "ds_read_b128 a[168:171], v50\n"
    "s_waitcnt lgkmcnt(5)\n"
    "v_mfma_f32_16x16x32_bf16 v[200:203], a[40:43], a[188:191], 0\n"
    "s_waitcnt lgkmcnt(4)\n"
    "v_cndmask_b32 v102, v102, v103, vcc\n"
    "v_mfma_f32_16x16x32_bf16 v[200:203], a[56:59], a[192:195], v[200:203]\n"
    "v_mfma_f32_16x16x32_bf16 a[24:27], a[12:15], v[36:39], a[24:27]\n"
    "buffer_store_dwordx4 a[12:15], v7, s[12:15], s86 offen sc1\n"
    "v_mfma_f32_16x16x32_bf16 v[160:163], a[32:35], a[188:191], 0\n"
    "s_cmp_eq_u32 s76, 2\n"
    "s_cselect_b32 s60, 52, s60\n"
    "v_cmp_eq_u32 vcc, 3, v17\n"
    "v_mfma_f32_16x16x32_bf16 v[160:163], a[48:51], a[192:195], v[160:163]\n"
    "v_mfma_f32_16x16x32_bf16 a[24:27], a[16:19], v[36:39], a[24:27]\n"
    "buffer_store_dwordx4 a[16:19], v7, s[12:15], s87 offen sc1\n"
    "ds_read_b128 a[172:175], v48 offset:20480\n"
    "v_mfma_f32_16x16x32_bf16 v[140:143], a[28:31], a[188:191], 0\n"
    "v_mov_b32 v103, s60\n"
    "s_lshr_b32 s60, s51, 2\n"
    "v_mfma_f32_16x16x32_bf16 v[140:143], a[44:47], a[192:195], v[140:143]\n"
    "v_mfma_f32_16x16x32_bf16 a[24:27], a[20:23], v[36:39], a[24:27]\n"
    "buffer_store_dwordx4 a[20:23], v7, s[12:15], s88 offen sc1\n"
    "v_lshl_add_u32 v120, v0, 1, s60\n"
    "v_mfma_f32_16x16x32_bf16 v[180:183], a[188:191], a[36:39], 0\n"
    "v_cndmask_b32 v102, v102, v103, vcc\n"
    "v_lshrrev_b32 v102, 1, v102\n"
    "v_mfma_f32_16x16x32_bf16 v[180:183], a[192:195], a[52:55], v[180:183]\n"
    "s_nop 1\n"
    "buffer_store_dwordx4 a[24:27], v43, s[12:15], s83 offen sc1\n"
    "ds_read_b128 a[176:179], v50 offset:20480\n"
    "s_waitcnt vmcnt(22) lgkmcnt(5)\n"
    "v_mfma_f32_16x16x32_bf16 v[184:187], a[72:75], a[156:159], v[184:187]\n"
    "s_waitcnt vmcnt(18) lgkmcnt(4)\n"
    "v_lshl_add_u32 v102, v102, 2, v20\n"
    "v_cmp_gt_u32 vcc, 0x4a, v0\n"
    "v_mfma_f32_16x16x32_bf16 v[184:187], a[88:91], a[160:163], v[184:187]\n"
    "buffer_load_dwordx4 a[136:139], v24, s[12:15], s59 offen sc1\n"
    "v_add_u32 v102, 0x94, v102\n"
    "v_mov_b32 v103, 0x60\n"
    "v_mfma_f32_16x16x32_bf16 v[144:147], a[64:67], a[156:159], v[144:147]\n"
    "s_waitcnt vmcnt(18)\n"
    "v_cndmask_b32 v102, v102, v120, vcc\n"
    "v_cmp_gt_u32 s[28:29], v103, v0\n"
    "v_mfma_f32_16x16x32_bf16 v[144:147], a[80:83], a[160:163], v[144:147]\n"
    "ds_read_b128 a[180:183], v48 offset:40960\n"
    "v_lshlrev_b32 v102, 10, v102\n"
    "s_nop 0\n"
    "v_mfma_f32_16x16x32_bf16 v[12:15], a[60:63], a[0:3], v[12:15]\n"
    "s_waitcnt vmcnt(17)\n"
    "v_cndmask_b32 v30, v121, v102, s[28:29]\n"
    "v_lshl_add_u32 v102, v6, 1, s52\n"
    "v_mfma_f32_16x16x32_bf16 v[12:15], a[76:79], a[0:3], v[12:15]\n"
    "v_lshrrev_b32 v103, 3, v102\n"
    "v_and_b32 v102, 7, v102\n"
    "v_mfma_f32_16x16x32_bf16 v[164:167], a[156:159], a[68:71], v[164:167]\n"
    "buffer_load_dwordx4 a[128:131], v23, s[12:15], s59 offen offset:1024 sc1\n"
    "s_waitcnt vmcnt(17)\n"
    "v_lshl_add_u32 v102, v102, 3, v103\n"
    "v_mfma_f32_16x16x32_bf16 v[164:167], a[160:163], a[84:87], v[164:167]\n"
    "ds_read_b128 a[184:187], v50 offset:40960\n"
    "s_waitcnt lgkmcnt(5)\n"
    "s_and_b32 s60, s40, 0xc6\n"
    "v_mfma_f32_16x16x32_bf16 v[188:191], a[72:75], a[164:167], v[188:191]\n"
    "s_waitcnt lgkmcnt(4)\n"
    "v_add_u32 v102, s60, v102\n"
    "s_sub_u32 s60, s49, 2\n"
    "v_mfma_f32_16x16x32_bf16 v[188:191], a[88:91], a[168:171], v[188:191]\n"
    "s_cmp_eq_u32 s54, 0\n"
    "s_cselect_b32 s60, s49, s60\n"
    "v_mfma_f32_16x16x32_bf16 v[148:151], a[64:67], a[164:167], v[148:151]\n"
    "s_cmp_eq_u32 s52, 1\n"
    "s_cselect_b32 s60, s49, s60\n"
    "s_cselect_b32 s75, 49, 57\n"
    "v_mfma_f32_16x16x32_bf16 v[148:151], a[80:83], a[168:171], v[148:151]\n"
    "buffer_load_dwordx4 a[124:127], v23, s[12:15], s59 offen sc1\n"
    "ds_read_b128 a[188:191], v48 offset:61440\n"
    "s_lshr_b32 s76, s60, 2\n"
    "v_mfma_f32_16x16x32_bf16 v[128:131], a[60:63], a[164:167], v[128:131]\n"
    "s_lshl1_add_u32 s75, s76, s75\n"
    "s_and_b32 s60, s60, 3\n"
    "s_lshl_b32 s60, s60, 6\n"
    "v_mfma_f32_16x16x32_bf16 v[128:131], a[76:79], a[168:171], v[128:131]\n"
    "s_add_u32 s60, s60, s75\n"
    "v_cmp_gt_u32 vcc, 8, v0\n"
    "v_mov_b32 v103, s60\n"
    "v_mfma_f32_16x16x32_bf16 v[168:171], a[164:167], a[68:71], v[168:171]\n"
    "v_cmp_gt_u32 s[72:73], 9, v0\n"
    "v_cndmask_b32 v102, v103, v102, vcc\n"
    "v_mfma_f32_16x16x32_bf16 v[168:171], a[168:171], a[84:87], v[168:171]\n"
    "ds_read_b128 a[192:195], v50 offset:61440\n"
    "s_waitcnt lgkmcnt(5)\n"
    "v_lshlrev_b32 v102, 10, v102\n"
    "v_mfma_f32_16x16x32_bf16 v[192:195], a[72:75], a[172:175], v[192:195]\n"
    "buffer_load_dwordx4 a[132:135], v23, s[12:15], s59 offen offset:2048 sc1\n"
    "s_add_u32 s59, s59, 0x3c00\n"
    "s_waitcnt lgkmcnt(4)\n"
    "v_mfma_f32_16x16x32_bf16 v[192:195], a[88:91], a[176:179], v[192:195]\n"
    "v_cndmask_b32 v98, v121, v102, s[72:73]\n"
    "s_lshr_b32 s60, s54, 2\n"
    "v_mfma_f32_16x16x32_bf16 v[152:155], a[64:67], a[172:175], v[152:155]\n"
    "s_sub_u32 s60, s60, 2\n"
    "v_lshrrev_b32 v102, 1, v6\n"
    "v_add_u32 v102, s60, v102\n"
    "v_mfma_f32_16x16x32_bf16 v[152:155], a[80:83], a[176:179], v[152:155]\n"
    "ds_read_b128 a[156:159], v45 offset:256\n"
    "v_max_i32 v102, 0, v102\n"
    "s_lshl_b32 s60, s53, 14\n"
    "v_mfma_f32_16x16x32_bf16 v[132:135], a[60:63], a[172:175], v[132:135]\n"
    "s_add_u32 s60, s60, 0x8000\n"
    "v_and_b32 v103, 1, v6\n"
    "v_cmp_gt_u32 vcc, 36, v6\n"
    "v_mfma_f32_16x16x32_bf16 v[132:135], a[76:79], a[176:179], v[132:135]\n"
    "buffer_load_dwordx4 a[152:155], v24, s[12:15], s59 offen sc1\n"
    "v_lshl_add_u32 v102, v102, 2, v103\n"
    "v_lshl_add_u32 v102, v102, 4, s60\n"
    "v_mfma_f32_16x16x32_bf16 v[172:175], a[172:175], a[68:71], v[172:175]\n"
    "v_cndmask_b32 v99, v121, v102, vcc\n"
    "v_lshlrev_b32 v102, 2, v9\n"
    "v_mfma_f32_16x16x32_bf16 v[172:175], a[176:179], a[84:87], v[172:175]\n"
    "ds_read_b128 a[160:163], v47 offset:256\n"
    "s_waitcnt lgkmcnt(5)\n"
    "v_sub_u32 v102, v8, v102\n"
    "v_mfma_f32_16x16x32_bf16 v[196:199], a[72:75], a[180:183], v[196:199]\n"
    "s_waitcnt lgkmcnt(4)\n"
    "v_mov_b32 v121, 0xff800000\n"
    "v_mfma_f32_16x16x32_bf16 v[196:199], a[88:91], a[184:187], v[196:199]\n"
    "s_cmp_eq_u32 s54, 0\n"
    "s_cselect_b64 s[24:25], 0, -1\n"
    "v_subrev_u32 v103, 0, v102\n"
    "v_mfma_f32_16x16x32_bf16 v[156:159], a[64:67], a[180:183], v[156:159]\n"
    "buffer_load_dwordx4 a[144:147], v23, s[12:15], s59 offen offset:1024 sc1\n"
    "v_cmp_ge_u32 s[30:31], 7, v103\n"
    "v_cmp_ge_i32_e64 vcc, -9, v103\n"
    "v_mfma_f32_16x16x32_bf16 v[156:159], a[80:83], a[184:187], v[156:159]\n"
    "ds_read_b128 a[164:167], v44 offset:256\n"
    "v_mov_b32 v120, 0xffff\n"
    "v_cndmask_b32 v104, v121, 0, s[30:31]\n"
    "v_mfma_f32_16x16x32_bf16 v[136:139], a[60:63], a[180:183], v[136:139]\n"
    "v_cndmask_b32 v118, 0, v120, s[30:31]\n"
    "v_cndmask_b32 v108, v121, 0, vcc\n"
    "v_mfma_f32_16x16x32_bf16 v[136:139], a[76:79], a[184:187], v[136:139]\n"
    "v_cndmask_b32 v116, 0, v120, vcc\n"
    "v_cndmask_b32 v112, v121, v108, s[24:25]\n"
    "v_mfma_f32_16x16x32_bf16 v[176:179], a[180:183], a[68:71], v[176:179]\n"
    "v_subrev_u32 v103, 1, v102\n"
    "v_cmp_ge_u32 s[30:31], 7, v103\n"
    "v_mfma_f32_16x16x32_bf16 v[176:179], a[184:187], a[84:87], v[176:179]\n"
    "buffer_load_dwordx4 a[140:143], v23, s[12:15], s59 offen sc1\n"
    "ds_read_b128 a[168:171], v46 offset:256\n"
    "s_waitcnt lgkmcnt(5)\n"
    "v_mfma_f32_16x16x32_bf16 v[200:203], a[72:75], a[188:191], v[200:203]\n"
    "s_waitcnt lgkmcnt(4)\n"
    "v_cmp_ge_i32 vcc, -9, v103\n"
    "v_mov_b32 v120, 0xffff0000\n"
    "v_mfma_f32_16x16x32_bf16 v[200:203], a[88:91], a[192:195], v[200:203]\n"
    "v_cndmask_b32 v105, v121, 0, s[30:31]\n"
    "v_cndmask_b32 v122, 0, v120, s[30:31]\n"
    "v_mfma_f32_16x16x32_bf16 v[160:163], a[64:67], a[188:191], v[160:163]\n"
    "v_or_b32 v118, v118, v122\n"
    "v_cndmask_b32 v109, v121, 0, vcc\n"
    "v_mfma_f32_16x16x32_bf16 v[160:163], a[80:83], a[192:195], v[160:163]\n"
    "ds_read_b128 a[172:175], v44 offset:20736\n"
    "v_cndmask_b32 v122, 0, v120, vcc\n"
    "v_or_b32 v116, v116, v122\n"
    "v_mfma_f32_16x16x32_bf16 v[140:143], a[60:63], a[188:191], v[140:143]\n"
    "buffer_load_dwordx4 a[148:151], v23, s[12:15], s59 offen offset:2048 sc1\n"
    "s_add_u32 s59, s59, 0x3c00\n"
    "v_cndmask_b32 v113, v121, v109, s[24:25]\n"
    "v_mfma_f32_16x16x32_bf16 v[140:143], a[76:79], a[192:195], v[140:143]\n"
    "v_subrev_u32 v103, 2, v102\n"
    "v_cmp_ge_u32 s[30:31], 7, v103\n"
    "v_mfma_f32_16x16x32_bf16 v[180:183], a[188:191], a[68:71], v[180:183]\n"
    "v_cmp_ge_i32 vcc, -9, v103\n"
    "v_mov_b32 v120, 0xffff\n"
    "v_mfma_f32_16x16x32_bf16 v[180:183], a[192:195], a[84:87], v[180:183]\n"
    "ds_read_b128 a[176:179], v46 offset:20736\n"
    "s_waitcnt vmcnt(22) lgkmcnt(5)\n"
    "v_cndmask_b32 v106, v121, 0, s[30:31]\n"
    "s_waitcnt vmcnt(8)\n"
    "s_barrier\n"
    "s_cmp_lt_u32 s41, 2\n"
    "s_cbranch_scc0 .Lskip_9_%=\n"
    "buffer_store_dwordx2 v[18:19], v31, s[12:15], s89 offen sc1\n"
    ".Lskip_9_%=:\n"
    "v_mfma_f32_16x16x32_bf16 v[184:187], a[104:107], a[156:159], v[184:187]\n"
    "s_waitcnt lgkmcnt(4)\n"
    "v_cndmask_b32 v119, 0, v120, s[30:31]\n"
    "v_cndmask_b32 v110, v121, 0, vcc\n"
    "v_mfma_f32_16x16x32_bf16 v[184:187], a[120:123], a[160:163], v[184:187]\n"
    "buffer_load_dwordx4 a[40:43], v24, s[12:15], s59 offen sc1\n"
    "v_cndmask_b32 v117, 0, v120, vcc\n"
    "v_cndmask_b32 v114, v121, v110, s[24:25]\n"
    "v_mfma_f32_16x16x32_bf16 v[144:147], a[96:99], a[156:159], v[144:147]\n"
    "v_subrev_u32 v103, 3, v102\n"
    "v_cmp_ge_u32 s[30:31], 7, v103\n"
    "v_mfma_f32_16x16x32_bf16 v[144:147], a[112:115], a[160:163], v[144:147]\n"
    "ds_read_b128 a[180:183], v44 offset:41216\n"
    "v_cmp_ge_i32 vcc, -9, v103\n"
    "v_mov_b32 v120, 0xffff0000\n"
    "v_mfma_f32_16x16x32_bf16 v[12:15], a[92:95], a[0:3], v[12:15]\n"
    "v_cndmask_b32 v107, v121, 0, s[30:31]\n"
    "v_cndmask_b32 v122, 0, v120, s[30:31]\n"
    "v_mfma_f32_16x16x32_bf16 v[12:15], a[108:111], a[0:3], v[12:15]\n"
    "v_or_b32 v119, v119, v122\n"
    "v_cndmask_b32 v111, v121, 0, vcc\n"
    "v_mfma_f32_16x16x32_bf16 v[164:167], a[156:159], a[100:103], v[164:167]\n"
    "buffer_load_dwordx4 a[32:35], v23, s[12:15], s59 offen offset:1024 sc1\n"
    "v_cndmask_b32 v122, 0, v120, vcc\n"
    "v_or_b32 v117, v117, v122\n"
    "v_mfma_f32_16x16x32_bf16 v[164:167], a[160:163], a[116:119], v[164:167]\n"
    "ds_read_b128 a[184:187], v46 offset:41216\n"
    "s_waitcnt lgkmcnt(5)\n"
    "v_cndmask_b32 v115, v121, v111, s[24:25]\n"
    "v_mfma_f32_16x16x32_bf16 v[188:191], a[104:107], a[164:167], v[188:191]\n"
    "s_waitcnt lgkmcnt(4)\n"
    "s_mul_i32 s43, s41, 0xc00\n"
    "s_add_u32 s43, s43, 0x17000\n"
    "v_mfma_f32_16x16x32_bf16 v[188:191], a[120:123], a[168:171], v[188:191]\n"
    "v_lshl_add_u32 v123, v6, 3, s43\n"
    "v_cmp_eq_u32 vcc, 1, v9\n"
    "v_mfma_f32_16x16x32_bf16 v[148:151], a[96:99], a[164:167], v[148:151]\n"
    "v_lshl_add_u32 v102, v8, 3, s43\n"
    "v_mov_b32 v121, 0x40000\n"
    "v_mfma_f32_16x16x32_bf16 v[148:151], a[112:115], a[168:171], v[148:151]\n"
    "buffer_load_dwordx4 a[28:31], v23, s[12:15], s59 offen sc1\n"
    "ds_read_b128 a[188:191], v44 offset:61696\n"
    "v_cndmask_b32 v124, v121, v102, vcc\n"
    "v_mfma_f32_16x16x32_bf16 v[128:131], a[92:95], a[164:167], v[128:131]\n"
    "v_lshrrev_b32 v102, 2, v6\n"
    "v_lshl_add_u32 v125, v102, 3, s43\n"
    "v_mfma_f32_16x16x32_bf16 v[128:131], a[108:111], a[168:171], v[128:131]\n"
    "v_and_b32 v102, 1, v9\n"
    "v_mul_u32_u24 v102, 0x5000, v102\n"
    "v_mfma_f32_16x16x32_bf16 v[168:171], a[164:167], a[100:103], v[168:171]\n"
    "v_lshrrev_b32 v103, 1, v9\n"
    "v_lshl_add_u32 v102, v103, 8, v102\n"
    "v_mfma_f32_16x16x32_bf16 v[168:171], a[168:171], a[116:119], v[168:171]\n"
    "ds_read_b128 a[192:195], v46 offset:61696\n"
    "s_waitcnt lgkmcnt(5)\n"
    "v_lshl_add_u32 v126, v8, 4, v102\n"
    "v_mfma_f32_16x16x32_bf16 v[192:195], a[104:107], a[172:175], v[192:195]\n"
    "buffer_load_dwordx4 a[36:39], v23, s[12:15], s59 offen offset:2048 sc1\n"
    "s_add_u32 s59, s59, 0x3c00\n"
    "s_waitcnt lgkmcnt(4)\n"
    "v_mfma_f32_16x16x32_bf16 v[192:195], a[120:123], a[176:179], v[192:195]\n"
    "v_mul_u32_u24 v102, 0x5000, v9\n"
    "v_lshl_add_u32 v127, v8, 3, v102\n"
    "v_mfma_f32_16x16x32_bf16 v[152:155], a[96:99], a[172:175], v[152:155]\n"
    "s_and_b32 s70, s51, 1\n"
    "s_lshl4_add_u32 s60, s41, s54\n"
    "s_lshl1_add_u32 s60, s60, s53\n"
    "v_mfma_f32_16x16x32_bf16 v[152:155], a[112:115], a[176:179], v[152:155]\n"
    "ds_read_b128 a[156:159], v49 offset:256\n"
    "s_mul_i32 s64, s60, 0x500\n"
    "s_mul_i32 s60, s51, 0xa0\n"
    "v_mfma_f32_16x16x32_bf16 v[132:135], a[92:95], a[172:175], v[132:135]\n"
    "s_add_u32 s64, s64, s60\n"
    "s_mov_b32 s26, 0x3ea52b5c\n"
    "s_mul_i32 s65, s50, 0x23000\n"
    "v_mfma_f32_16x16x32_bf16 v[132:135], a[108:111], a[176:179], v[132:135]\n"
    "buffer_load_dwordx4 a[56:59], v24, s[12:15], s59 offen sc1\n"
    "s_add_u32 s65, s65, 0x3e4000\n"
    "s_lshl1_add_u32 s60, s49, s52\n"
    "v_mfma_f32_16x16x32_bf16 v[172:175], a[172:175], a[100:103], v[172:175]\n"
    "s_mul_i32 s60, s60, 0x14000\n"
    "s_add_u32 s60, s60, 0x61c000\n"
    "s_lshl2_add_u32 s76, s51, s41\n"
    "v_mfma_f32_16x16x32_bf16 v[172:175], a[176:179], a[116:119], v[172:175]\n"
    "ds_read_b128 a[160:163], v51 offset:256\n"
    "s_waitcnt lgkmcnt(5)\n"
    "s_lshl_b32 s75, s76, 9\n"
    "v_mfma_f32_16x16x32_bf16 v[196:199], a[104:107], a[180:183], v[196:199]\n"
    "s_waitcnt lgkmcnt(4)\n"
    "s_add_u32 s67, s60, s75\n"
    "s_add_u32 s68, s67, 0xa000\n"
    "v_mfma_f32_16x16x32_bf16 v[196:199], a[120:123], a[184:187], v[196:199]\n"
    "s_lshl_b32 s75, s76, 7\n"
    "s_add_u32 s69, s60, s75\n"
    "s_add_u32 s69, s69, 0x4000\n"
    "v_mfma_f32_16x16x32_bf16 v[156:159], a[96:99], a[180:183], v[156:159]\n"
    "buffer_load_dwordx4 a[48:51], v23, s[12:15], s59 offen offset:1024 sc1\n"
    "s_mul_i32 s60, s51, 10\n"
    "s_lshl4_add_u32 s76, s41, 8\n"
    "v_mfma_f32_16x16x32_bf16 v[156:159], a[112:115], a[184:187], v[156:159]\n"
    "ds_read_b128 a[164:167], v48 offset:256\n"
    "v_add_u32 v122, s76, v8\n"
    "v_mul_u32_u24 v122, 0x500, v122\n"
    "v_mfma_f32_16x16x32_bf16 v[136:139], a[92:95], a[180:183], v[136:139]\n"
    "v_xor_b32 v121, 8, v8\n"
    "s_lshl1_add_u32 s76, s70, s60\n"
    "v_mfma_f32_16x16x32_bf16 v[136:139], a[108:111], a[184:187], v[136:139]\n"
    "v_add3_u32 v102, v9, s76, 0\n"
    "v_xor_b32 v102, v102, v121\n"
    "v_mfma_f32_16x16x32_bf16 v[176:179], a[180:183], a[100:103], v[176:179]\n"
    "v_lshl_add_u32 v102, v102, 4, v122\n"
    "ds_read_b128 v[212:215], v102\n"
    "v_mfma_f32_16x16x32_bf16 v[176:179], a[184:187], a[116:119], v[176:179]\n"
    "buffer_load_dwordx4 a[44:47], v23, s[12:15], s59 offen sc1\n"
    "ds_read_b128 a[168:171], v50 offset:256\n"
    "s_waitcnt lgkmcnt(6)\n"
    "v_mfma_f32_16x16x32_bf16 v[200:203], a[104:107], a[188:191], v[200:203]\n"
    "s_waitcnt lgkmcnt(5)\n"
    "v_add3_u32 v102, v9, s76, 4\n"
    "v_xor_b32 v102, v102, v121\n"
    "v_mfma_f32_16x16x32_bf16 v[200:203], a[120:123], a[192:195], v[200:203]\n"
    "v_lshl_add_u32 v102, v102, 4, v122\n"
    "ds_read_b128 v[216:219], v102\n"
    "v_mfma_f32_16x16x32_bf16 v[160:163], a[96:99], a[188:191], v[160:163]\n"
    "s_xor_b32 s76, s70, 1\n"
    "s_lshl3_add_u32 s76, s76, s60\n"
    "v_lshrrev_b32 v102, 1, v9\n"
    "v_mfma_f32_16x16x32_bf16 v[160:163], a[112:115], a[192:195], v[160:163]\n"
    "ds_read_b128 a[172:175], v48 offset:20736\n"
    "v_add_u32 v102, s76, v102\n"
    "v_and_b32_e64 v103, 1, v9\n"
    "v_mfma_f32_16x16x32_bf16 v[140:143], a[92:95], a[188:191], v[140:143]\n"
    "buffer_load_dwordx4 a[52:55], v23, s[12:15], s59 offen offset:2048 sc1\n"
    "s_add_u32 s59, s59, 0x3c00\n"
    "v_lshl_add_u32 v122, v103, 3, v122\n"
    "v_mfma_f32_16x16x32_bf16 v[140:143], a[108:111], a[192:195], v[140:143]\n"
    "v_xor_b32 v102, v102, v121\n"
    "v_lshl_add_u32 v102, v102, 4, v122\n"
    "v_mfma_f32_16x16x32_bf16 v[180:183], a[188:191], a[100:103], v[180:183]\n"
    "ds_read_b64 v[220:221], v102\n"
    "v_mfma_f32_16x16x32_bf16 v[180:183], a[192:195], a[116:119], v[180:183]\n"
    "ds_read_b128 a[176:179], v50 offset:20736\n"
    "s_waitcnt vmcnt(15) lgkmcnt(8)\n"
    "v_mfma_f32_16x16x32_bf16 v[184:187], a[136:139], a[156:159], v[184:187]\n"
    "s_waitcnt vmcnt(11) lgkmcnt(7)\n"
    "v_mfma_f32_16x16x32_bf16 v[184:187], a[152:155], a[160:163], v[184:187]\n"
    "buffer_load_dwordx4 a[72:75], v24, s[12:15], s59 offen sc1\n"
    "v_mfma_f32_16x16x32_bf16 v[144:147], a[128:131], a[156:159], v[144:147]\n"
    "s_waitcnt vmcnt(11)\n"
    "v_mfma_f32_16x16x32_bf16 v[144:147], a[144:147], a[160:163], v[144:147]\n"
    "ds_read_b128 a[180:183], v48 offset:41216\n"
    "v_mfma_f32_16x16x32_bf16 v[12:15], a[124:127], a[0:3], v[12:15]\n"
    "s_waitcnt vmcnt(10)\n"
    "v_mfma_f32_16x16x32_bf16 v[12:15], a[140:143], a[0:3], v[12:15]\n"
    "v_mfma_f32_16x16x32_bf16 v[164:167], a[156:159], a[132:135], v[164:167]\n"
    "buffer_load_dwordx4 a[64:67], v23, s[12:15], s59 offen offset:1024 sc1\n"
    "s_waitcnt vmcnt(10)\n"
    "v_mfma_f32_16x16x32_bf16 v[164:167], a[160:163], a[148:151], v[164:167]\n"
    "ds_read_b128 a[184:187], v50 offset:41216\n"
    "s_waitcnt lgkmcnt(8)\n"
    "v_mfma_f32_16x16x32_bf16 v[188:191], a[136:139], a[164:167], v[188:191]\n"
    "s_waitcnt lgkmcnt(6)\n"
    "v_mfma_f32_16x16x32_bf16 v[188:191], a[152:155], a[168:171], v[188:191]\n"
    "v_mfma_f32_16x16x32_bf16 v[148:151], a[128:131], a[164:167], v[148:151]\n"
    "v_mfma_f32_16x16x32_bf16 v[148:151], a[144:147], a[168:171], v[148:151]\n"
    "buffer_load_dwordx4 a[60:63], v23, s[12:15], s59 offen sc1\n"
    "ds_read_b128 a[188:191], v48 offset:61696\n"
    "v_mfma_f32_16x16x32_bf16 v[128:131], a[124:127], a[164:167], v[128:131]\n"
    "v_mfma_f32_16x16x32_bf16 v[128:131], a[140:143], a[168:171], v[128:131]\n"
    "v_mfma_f32_16x16x32_bf16 v[168:171], a[164:167], a[132:135], v[168:171]\n"
    "v_mfma_f32_16x16x32_bf16 v[168:171], a[168:171], a[148:151], v[168:171]\n"
    "ds_read_b128 a[192:195], v50 offset:61696\n"
    "s_waitcnt lgkmcnt(6)\n"
    "v_mfma_f32_16x16x32_bf16 v[192:195], a[136:139], a[172:175], v[192:195]\n"
    "buffer_load_dwordx4 a[68:71], v23, s[12:15], s59 offen offset:2048 sc1\n"
    "s_add_u32 s59, s59, 0x3c00\n"
    "s_waitcnt lgkmcnt(4)\n"
    "v_mfma_f32_16x16x32_bf16 v[192:195], a[152:155], a[176:179], v[192:195]\n"
    "v_mfma_f32_16x16x32_bf16 v[152:155], a[128:131], a[172:175], v[152:155]\n"
    "v_mfma_f32_16x16x32_bf16 v[152:155], a[144:147], a[176:179], v[152:155]\n"
    "ds_read_b128 a[156:159], v45 offset:512\n"
    "v_mfma_f32_16x16x32_bf16 v[132:135], a[124:127], a[172:175], v[132:135]\n"
    "v_mfma_f32_16x16x32_bf16 v[132:135], a[140:143], a[176:179], v[132:135]\n"
    "buffer_load_dwordx4 a[88:91], v24, s[12:15], s59 offen sc1\n"
    "v_mfma_f32_16x16x32_bf16 v[172:175], a[172:175], a[132:135], v[172:175]\n"
    "v_mfma_f32_16x16x32_bf16 v[172:175], a[176:179], a[148:151], v[172:175]\n"
    "ds_read_b128 a[160:163], v47 offset:512\n"
    "s_waitcnt lgkmcnt(5)\n"
    "v_mfma_f32_16x16x32_bf16 v[196:199], a[136:139], a[180:183], v[196:199]\n"
    "s_waitcnt lgkmcnt(4)\n"
    "v_mfma_f32_16x16x32_bf16 v[196:199], a[152:155], a[184:187], v[196:199]\n"
    "buffer_load_dwordx4 a[80:83], v23, s[12:15], s59 offen offset:1024 sc1\n"
    "v_mfma_f32_16x16x32_bf16 v[156:159], a[128:131], a[180:183], v[156:159]\n"
    "v_mfma_f32_16x16x32_bf16 v[156:159], a[144:147], a[184:187], v[156:159]\n"
    "ds_read_b128 a[164:167], v44 offset:512\n"
    "v_mfma_f32_16x16x32_bf16 v[136:139], a[124:127], a[180:183], v[136:139]\n"
    "v_mfma_f32_16x16x32_bf16 v[136:139], a[140:143], a[184:187], v[136:139]\n"
    "buffer_load_dwordx4 a[76:79], v23, s[12:15], s59 offen sc1\n"
    "v_mfma_f32_16x16x32_bf16 v[176:179], a[180:183], a[132:135], v[176:179]\n"
    "v_mfma_f32_16x16x32_bf16 v[176:179], a[184:187], a[148:151], v[176:179]\n"
    "ds_read_b128 a[168:171], v46 offset:512\n"
    "s_waitcnt lgkmcnt(5)\n"
    "v_mfma_f32_16x16x32_bf16 v[200:203], a[136:139], a[188:191], v[200:203]\n"
    "s_waitcnt lgkmcnt(4)\n"
    "v_mfma_f32_16x16x32_bf16 v[200:203], a[152:155], a[192:195], v[200:203]\n"
    "buffer_load_dwordx4 a[84:87], v23, s[12:15], s59 offen offset:2048 sc1\n"
    "s_add_u32 s59, s59, 0x3c00\n"
    "v_mfma_f32_16x16x32_bf16 v[160:163], a[128:131], a[188:191], v[160:163]\n"
    "v_mfma_f32_16x16x32_bf16 v[160:163], a[144:147], a[192:195], v[160:163]\n"
    "ds_read_b128 a[172:175], v44 offset:20992\n"
    "v_mfma_f32_16x16x32_bf16 v[140:143], a[124:127], a[188:191], v[140:143]\n"
    "v_mfma_f32_16x16x32_bf16 v[140:143], a[140:143], a[192:195], v[140:143]\n"
    "s_cmp_lt_u32 s41, 1\n"
    "s_cbranch_scc0 .Lskip_10_%=\n"
    "buffer_load_dwordx2 v[96:97], v98, s[12:15], s61 offen sc1\n"
    ".Lskip_10_%=:\n"
    "v_mfma_f32_16x16x32_bf16 v[180:183], a[188:191], a[132:135], v[180:183]\n"
    "v_mfma_f32_16x16x32_bf16 v[180:183], a[192:195], a[148:151], v[180:183]\n"
    "ds_read_b128 a[176:179], v46 offset:20992\n"
    "s_waitcnt vmcnt(15) lgkmcnt(5)\n"
    "v_mfma_f32_16x16x32_bf16 v[184:187], a[40:43], a[156:159], v[184:187]\n"
    "s_waitcnt vmcnt(11) lgkmcnt(4)\n"
    "v_mfma_f32_16x16x32_bf16 v[184:187], a[56:59], a[160:163], v[184:187]\n"
    "buffer_load_dwordx4 a[104:107], v24, s[12:15], s59 offen sc1\n"
    "v_mfma_f32_16x16x32_bf16 v[144:147], a[32:35], a[156:159], v[144:147]\n"
    "s_waitcnt vmcnt(11)\n"
    "v_mfma_f32_16x16x32_bf16 v[144:147], a[48:51], a[160:163], v[144:147]\n"
    "ds_read_b128 a[180:183], v44 offset:41472\n"
    "v_mfma_f32_16x16x32_bf16 v[12:15], a[28:31], a[0:3], v[12:15]\n"
    "s_waitcnt vmcnt(10)\n"
    "v_mfma_f32_16x16x32_bf16 v[12:15], a[44:47], a[0:3], v[12:15]\n"
    "buffer_load_dwordx4 a[96:99], v23, s[12:15], s59 offen offset:1024 sc1\n"
    "v_mfma_f32_16x16x32_bf16 v[164:167], a[156:159], a[36:39], v[164:167]\n"
    "s_waitcnt vmcnt(10)\n"
    "v_mfma_f32_16x16x32_bf16 v[164:167], a[160:163], a[52:55], v[164:167]\n"
    "ds_read_b128 a[184:187], v46 offset:41472\n"
    "s_waitcnt lgkmcnt(5)\n"
    "v_mfma_f32_16x16x32_bf16 v[188:191], a[40:43], a[164:167], v[188:191]\n"
    "s_waitcnt lgkmcnt(4)\n"
    "v_mfma_f32_16x16x32_bf16 v[188:191], a[56:59], a[168:171], v[188:191]\n"
    "buffer_load_dwordx4 a[92:95], v23, s[12:15], s59 offen sc1\n"
    "v_mfma_f32_16x16x32_bf16 v[148:151], a[32:35], a[164:167], v[148:151]\n"
    "v_mfma_f32_16x16x32_bf16 v[148:151], a[48:51], a[168:171], v[148:151]\n"
    "ds_read_b128 a[188:191], v44 offset:61952\n"
    "v_mfma_f32_16x16x32_bf16 v[128:131], a[28:31], a[164:167], v[128:131]\n"
    "v_mfma_f32_16x16x32_bf16 v[128:131], a[44:47], a[168:171], v[128:131]\n"
    "buffer_load_dwordx4 a[100:103], v23, s[12:15], s59 offen offset:2048 sc1\n"
    "s_add_u32 s59, s59, 0x3c00\n"
    "v_mfma_f32_16x16x32_bf16 v[168:171], a[164:167], a[36:39], v[168:171]\n"
    "v_mfma_f32_16x16x32_bf16 v[168:171], a[168:171], a[52:55], v[168:171]\n"
    "ds_read_b128 a[192:195], v46 offset:61952\n"
    "s_waitcnt lgkmcnt(5)\n"
    "v_mfma_f32_16x16x32_bf16 v[192:195], a[40:43], a[172:175], v[192:195]\n"
    "s_waitcnt lgkmcnt(4)\n"
    "v_mfma_f32_16x16x32_bf16 v[192:195], a[56:59], a[176:179], v[192:195]\n"
    "buffer_load_dwordx4 a[120:123], v24, s[12:15], s59 offen sc1\n"
    "v_mfma_f32_16x16x32_bf16 v[152:155], a[32:35], a[172:175], v[152:155]\n"
    "v_mfma_f32_16x16x32_bf16 v[152:155], a[48:51], a[176:179], v[152:155]\n"
    "ds_read_b128 a[156:159], v49 offset:512\n"
    "v_mfma_f32_16x16x32_bf16 v[132:135], a[28:31], a[172:175], v[132:135]\n"
    "v_mfma_f32_16x16x32_bf16 v[132:135], a[44:47], a[176:179], v[132:135]\n"
    "buffer_load_dwordx4 a[112:115], v23, s[12:15], s59 offen offset:1024 sc1\n"
    "v_mfma_f32_16x16x32_bf16 v[172:175], a[172:175], a[36:39], v[172:175]\n"
    "v_mfma_f32_16x16x32_bf16 v[172:175], a[176:179], a[52:55], v[172:175]\n"
    "ds_read_b128 a[160:163], v51 offset:512\n"
    "s_waitcnt lgkmcnt(5)\n"
    "v_mfma_f32_16x16x32_bf16 v[196:199], a[40:43], a[180:183], v[196:199]\n"
    "s_waitcnt lgkmcnt(4)\n"
    "v_mfma_f32_16x16x32_bf16 v[196:199], a[56:59], a[184:187], v[196:199]\n"
    "buffer_load_dwordx4 a[108:111], v23, s[12:15], s59 offen sc1\n"
    "v_mfma_f32_16x16x32_bf16 v[156:159], a[32:35], a[180:183], v[156:159]\n"
    "v_mfma_f32_16x16x32_bf16 v[156:159], a[48:51], a[184:187], v[156:159]\n"
    "ds_read_b128 a[164:167], v48 offset:512\n"
    "v_mfma_f32_16x16x32_bf16 v[136:139], a[28:31], a[180:183], v[136:139]\n"
    "v_mfma_f32_16x16x32_bf16 v[136:139], a[44:47], a[184:187], v[136:139]\n"
    "buffer_load_dwordx4 a[116:119], v23, s[12:15], s59 offen offset:2048 sc1\n"
    "s_add_u32 s59, s59, 0x3c00\n"
    "v_mfma_f32_16x16x32_bf16 v[176:179], a[180:183], a[36:39], v[176:179]\n"
    "v_mfma_f32_16x16x32_bf16 v[176:179], a[184:187], a[52:55], v[176:179]\n"
    "ds_read_b128 a[168:171], v50 offset:512\n"
    "s_waitcnt lgkmcnt(5)\n"
    "v_mfma_f32_16x16x32_bf16 v[200:203], a[40:43], a[188:191], v[200:203]\n"
    "s_waitcnt lgkmcnt(4)\n"
    "v_mfma_f32_16x16x32_bf16 v[200:203], a[56:59], a[192:195], v[200:203]\n"
    "buffer_load_dwordx4 a[136:139], v24, s[12:15], s59 offen sc1\n"
    "v_mfma_f32_16x16x32_bf16 v[160:163], a[32:35], a[188:191], v[160:163]\n"
    "v_mfma_f32_16x16x32_bf16 v[160:163], a[48:51], a[192:195], v[160:163]\n"
    "ds_read_b128 a[172:175], v48 offset:20992\n"
    "v_mfma_f32_16x16x32_bf16 v[140:143], a[28:31], a[188:191], v[140:143]\n"
    "v_mfma_f32_16x16x32_bf16 v[140:143], a[44:47], a[192:195], v[140:143]\n"
    "buffer_load_dwordx4 a[128:131], v23, s[12:15], s59 offen offset:1024 sc1\n"
    "v_mfma_f32_16x16x32_bf16 v[180:183], a[188:191], a[36:39], v[180:183]\n"
    "v_mfma_f32_16x16x32_bf16 v[180:183], a[192:195], a[52:55], v[180:183]\n"
    "ds_read_b128 a[176:179], v50 offset:20992\n"
    "s_waitcnt vmcnt(17) lgkmcnt(5)\n"
    "s_waitcnt vmcnt(10)\n"
    "v_cmp_ne_u64 s[34:35], s[38:39], v[96:97]\n"
    "s_nop 0\n"
    "s_and_b64 s[34:35], s[34:35], s[72:73]\n"
    "s_cbranch_scc0 .Lgok_11_%=\n"
    "s_movk_i32 s46, 0x4e20\n"
    ".Lgspin_12_%=:\n"
    "s_sleep 0\n"
    "buffer_load_dwordx2 v[96:97], v98, s[12:15], s61 offen sc1\n"
    "s_sub_u32 s46, s46, 1\n"
    "s_cbranch_scc1 .Lgok_11_%=\n"
    "s_waitcnt vmcnt(0)\n"
    "v_cmp_ne_u64 s[34:35], s[38:39], v[96:97]\n"
    "s_nop 0\n"
    "s_and_b64 s[34:35], s[34:35], s[72:73]\n"
    "s_cbranch_scc1 .Lgspin_12_%=\n"
    ".Lgok_11_%=:\n"
    "s_mov_b32 m0, 0x16d40\n"
    "s_cmp_lt_u32 s41, 1\n"
    "s_cbranch_scc0 .Lskip_13_%=\n"
    "buffer_load_dwordx4 v99, s[12:15], 0 offen sc1 lds\n"
    ".Lskip_13_%=:\n"
    "v_mfma_f32_16x16x32_bf16 v[184:187], a[72:75], a[156:159], v[184:187]\n"
    "s_waitcnt lgkmcnt(4)\n"
    "v_mfma_f32_16x16x32_bf16 v[184:187], a[88:91], a[160:163], v[184:187]\n"
    "buffer_load_dwordx4 a[124:127], v23, s[12:15], s59 offen sc1\n"
    "v_mfma_f32_16x16x32_bf16 v[144:147], a[64:67], a[156:159], v[144:147]\n"
    "v_mfma_f32_16x16x32_bf16 v[144:147], a[80:83], a[160:163], v[144:147]\n"
    "ds_read_b128 a[180:183], v48 offset:41472\n"
    "v_mfma_f32_16x16x32_bf16 v[12:15], a[60:63], a[0:3], v[12:15]\n"
    "buffer_load_dwordx4 a[132:135], v23, s[12:15], s59 offen offset:2048 sc1\n"
    "s_add_u32 s59, s59, 0x3c00\n"
    "v_mfma_f32_16x16x32_bf16 v[12:15], a[76:79], a[0:3], v[12:15]\n"
    "v_mfma_f32_16x16x32_bf16 v[164:167], a[156:159], a[68:71], v[164:167]\n"
    "v_mfma_f32_16x16x32_bf16 v[164:167], a[160:163], a[84:87], v[164:167]\n"
    "buffer_load_dwordx4 a[152:155], v24, s[12:15], s59 offen sc1\n"
    "ds_read_b128 a[184:187], v50 offset:41472\n"
    "s_waitcnt lgkmcnt(5)\n"
    "v_mfma_f32_16x16x32_bf16 v[188:191], a[72:75], a[164:167], v[188:191]\n"
    "s_waitcnt lgkmcnt(4)\n"
    "v_mfma_f32_16x16x32_bf16 v[188:191], a[88:91], a[168:171], v[188:191]\n"
    "v_mfma_f32_16x16x32_bf16 v[148:151], a[64:67], a[164:167], v[148:151]\n"
    "v_mfma_f32_16x16x32_bf16 v[148:151], a[80:83], a[168:171], v[148:151]\n"
    "buffer_load_dwordx4 a[144:147], v23, s[12:15], s59 offen offset:1024 sc1\n"
    "ds_read_b128 a[188:191], v48 offset:61952\n"
    "v_mfma_f32_16x16x32_bf16 v[128:131], a[60:63], a[164:167], v[128:131]\n"
    "v_mfma_f32_16x16x32_bf16 v[128:131], a[76:79], a[168:171], v[128:131]\n"
    "v_mfma_f32_16x16x32_bf16 v[168:171], a[164:167], a[68:71], v[168:171]\n"
    "buffer_load_dwordx4 a[140:143], v23, s[12:15], s59 offen sc1\n"
    "v_mfma_f32_16x16x32_bf16 v[168:171], a[168:171], a[84:87], v[168:171]\n"
    "ds_read_b128 a[192:195], v50 offset:61952\n"
    "s_waitcnt lgkmcnt(5)\n"
    "v_mfma_f32_16x16x32_bf16 v[192:195], a[72:75], a[172:175], v[192:195]\n"
    "s_waitcnt lgkmcnt(4)\n"
    "v_mfma_f32_16x16x32_bf16 v[192:195], a[88:91], a[176:179], v[192:195]\n"
    "s_cmp_lt_u32 s41, 2\n"
    "s_cbranch_scc0 .Lskip_14_%=\n"
    "buffer_load_dwordx2 v[32:33], v30, s[12:15], s61 offen sc1\n"
    ".Lskip_14_%=:\n"
    "v_mfma_f32_16x16x32_bf16 v[152:155], a[64:67], a[172:175], v[152:155]\n"
    "v_mfma_f32_16x16x32_bf16 v[152:155], a[80:83], a[176:179], v[152:155]\n"
    "ds_read_b128 a[156:159], v45 offset:768\n"
    "v_mfma_f32_16x16x32_bf16 v[132:135], a[60:63], a[172:175], v[132:135]\n"
    "v_mfma_f32_16x16x32_bf16 v[132:135], a[76:79], a[176:179], v[132:135]\n"
    "buffer_load_dwordx4 a[148:151], v23, s[12:15], s59 offen offset:2048 sc1\n"
    "s_add_u32 s59, s59, 0x3c00\n"
    "v_mfma_f32_16x16x32_bf16 v[172:175], a[172:175], a[68:71], v[172:175]\n"
    "v_mfma_f32_16x16x32_bf16 v[172:175], a[176:179], a[84:87], v[172:175]\n"
    "ds_read_b128 a[160:163], v47 offset:768\n"
    "s_waitcnt lgkmcnt(5)\n"
    "v_mfma_f32_16x16x32_bf16 v[196:199], a[72:75], a[180:183], v[196:199]\n"
    "s_waitcnt lgkmcnt(4)\n"
    "v_mfma_f32_16x16x32_bf16 v[196:199], a[88:91], a[184:187], v[196:199]\n"
    "buffer_load_dwordx4 a[40:43], v24, s[12:15], s59 offen sc1\n"
    "v_mfma_f32_16x16x32_bf16 v[156:159], a[64:67], a[180:183], v[156:159]\n"
    "v_mfma_f32_16x16x32_bf16 v[156:159], a[80:83], a[184:187], v[156:159]\n"
    "ds_read_b128 a[164:167], v44 offset:768\n"
    "v_mfma_f32_16x16x32_bf16 v[136:139], a[60:63], a[180:183], v[136:139]\n"
    "v_mfma_f32_16x16x32_bf16 v[136:139], a[76:79], a[184:187], v[136:139]\n"
    "buffer_load_dwordx4 a[32:35], v23, s[12:15], s59 offen offset:1024 sc1\n"
    "v_mfma_f32_16x16x32_bf16 v[176:179], a[180:183], a[68:71], v[176:179]\n"
    "v_mfma_f32_16x16x32_bf16 v[176:179], a[184:187], a[84:87], v[176:179]\n"
    "ds_read_b128 a[168:171], v46 offset:768\n"
    "s_waitcnt lgkmcnt(5)\n"
    "v_mfma_f32_16x16x32_bf16 v[200:203], a[72:75], a[188:191], v[200:203]\n"
    "s_waitcnt lgkmcnt(4)\n"
    "v_mfma_f32_16x16x32_bf16 v[200:203], a[88:91], a[192:195], v[200:203]\n"
    "buffer_load_dwordx4 a[28:31], v23, s[12:15], s59 offen sc1\n"
    "v_mfma_f32_16x16x32_bf16 v[160:163], a[64:67], a[188:191], v[160:163]\n"
    "v_mfma_f32_16x16x32_bf16 v[160:163], a[80:83], a[192:195], v[160:163]\n"
    "ds_read_b128 a[172:175], v44 offset:21248\n"
    "v_mfma_f32_16x16x32_bf16 v[140:143], a[60:63], a[188:191], v[140:143]\n"
    "v_mfma_f32_16x16x32_bf16 v[140:143], a[76:79], a[192:195], v[140:143]\n"
    "buffer_load_dwordx4 a[36:39], v23, s[12:15], s59 offen offset:2048 sc1\n"
    "s_add_u32 s59, s59, 0x3c00\n"
    "v_mfma_f32_16x16x32_bf16 v[180:183], a[188:191], a[68:71], v[180:183]\n"
    "v_mfma_f32_16x16x32_bf16 v[180:183], a[192:195], a[84:87], v[180:183]\n"
    "ds_read_b128 a[176:179], v46 offset:21248\n"
    "s_waitcnt vmcnt(19) lgkmcnt(5)\n"
    "v_mfma_f32_16x16x32_bf16 v[184:187], a[104:107], a[156:159], v[184:187]\n"
    "s_waitcnt vmcnt(15) lgkmcnt(4)\n"
    "v_mfma_f32_16x16x32_bf16 v[184:187], a[120:123], a[160:163], v[184:187]\n"
    "buffer_load_dwordx4 a[56:59], v24, s[12:15], s59 offen sc1\n"
    "v_mfma_f32_16x16x32_bf16 v[144:147], a[96:99], a[156:159], v[144:147]\n"
    "s_waitcnt vmcnt(15)\n"
    "v_mfma_f32_16x16x32_bf16 v[144:147], a[112:115], a[160:163], v[144:147]\n"
    "ds_read_b128 a[180:183], v44 offset:41728\n"
    "v_mfma_f32_16x16x32_bf16 v[12:15], a[92:95], a[0:3], v[12:15]\n"
    "s_waitcnt vmcnt(14)\n"
    "v_mfma_f32_16x16x32_bf16 v[12:15], a[108:111], a[0:3], v[12:15]\n"
    "buffer_load_dwordx4 a[48:51], v23, s[12:15], s59 offen offset:1024 sc1\n"
    "v_mfma_f32_16x16x32_bf16 v[164:167], a[156:159], a[100:103], v[164:167]\n"
    "s_waitcnt vmcnt(14)\n"
    "v_mfma_f32_16x16x32_bf16 v[164:167], a[160:163], a[116:119], v[164:167]\n"
    "ds_read_b128 a[184:187], v46 offset:41728\n"
    "s_waitcnt lgkmcnt(5)\n"
    "v_mfma_f32_16x16x32_bf16 v[188:191], a[104:107], a[164:167], v[188:191]\n"
    "s_waitcnt lgkmcnt(4)\n"
    "v_mfma_f32_16x16x32_bf16 v[188:191], a[120:123], a[168:171], v[188:191]\n"
    "buffer_load_dwordx4 a[44:47], v23, s[12:15], s59 offen sc1\n"
    "v_mfma_f32_16x16x32_bf16 v[148:151], a[96:99], a[164:167], v[148:151]\n"
    "v_mfma_f32_16x16x32_bf16 v[148:151], a[112:115], a[168:171], v[148:151]\n"
    "ds_read_b128 a[188:191], v44 offset:62208\n"
    "v_mfma_f32_16x16x32_bf16 v[128:131], a[92:95], a[164:167], v[128:131]\n"
    "v_mfma_f32_16x16x32_bf16 v[128:131], a[108:111], a[168:171], v[128:131]\n"
    "buffer_load_dwordx4 a[52:55], v23, s[12:15], s59 offen offset:2048 sc1\n"
    "s_add_u32 s59, s59, 0x3c00\n"
    "v_mfma_f32_16x16x32_bf16 v[168:171], a[164:167], a[100:103], v[168:171]\n"
    "v_mfma_f32_16x16x32_bf16 v[168:171], a[168:171], a[116:119], v[168:171]\n"
    "ds_read_b128 a[192:195], v46 offset:62208\n"
    "s_waitcnt lgkmcnt(5)\n"
    "v_mfma_f32_16x16x32_bf16 v[192:195], a[104:107], a[172:175], v[192:195]\n"
    "s_waitcnt lgkmcnt(4)\n"
    "v_mfma_f32_16x16x32_bf16 v[192:195], a[120:123], a[176:179], v[192:195]\n"
    "buffer_load_dwordx4 a[72:75], v24, s[12:15], s59 offen sc1\n"
    "v_mfma_f32_16x16x32_bf16 v[152:155], a[96:99], a[172:175], v[152:155]\n"
    "v_mfma_f32_16x16x32_bf16 v[152:155], a[112:115], a[176:179], v[152:155]\n"
    "ds_read_b128 a[156:159], v49 offset:768\n"
    "s_waitcnt vmcnt(15)\n"
    "s_waitcnt vmcnt(10)\n"
    "v_cmp_ne_u64 s[34:35], s[38:39], v[32:33]\n"
    "s_nop 0\n"
    "s_and_b64 s[34:35], s[34:35], s[28:29]\n"
    "s_cbranch_scc0 .Lgok_15_%=\n"
    "s_movk_i32 s46, 0x4e20\n"
    ".Lgspin_16_%=:\n"
    "s_sleep 0\n"
    "buffer_load_dwordx2 v[32:33], v30, s[12:15], s61 offen sc1\n"
    "s_sub_u32 s46, s46, 1\n"
    "s_cbranch_scc1 .Lgok_15_%=\n"
    "s_waitcnt vmcnt(0)\n"
    "v_cmp_ne_u64 s[34:35], s[38:39], v[32:33]\n"
    "s_nop 0\n"
    "s_and_b64 s[34:35], s[34:35], s[28:29]\n"
    "s_cbranch_scc1 .Lgspin_16_%=\n"
    ".Lgok_15_%=:\n"
    "s_barrier\n"
    "v_mfma_f32_16x16x32_bf16 v[132:135], a[92:95], a[172:175], v[132:135]\n"
    "v_lshl_add_u32 v102, v17, 3, v20\n"
    "v_lshlrev_b32 v102, 2, v102\n"
    "v_mfma_f32_16x16x32_bf16 v[132:135], a[108:111], a[176:179], v[132:135]\n"
    "buffer_load_dwordx4 a[64:67], v23, s[12:15], s59 offen offset:1024 sc1\n"
    "v_add_u32 v102, 0x16d00, v102\n"
    "v_lshlrev_b32 v103, 5, v9\n"
    "v_mfma_f32_16x16x32_bf16 v[172:175], a[172:175], a[100:103], v[172:175]\n"
    "v_add_u32 v103, 0x16d00, v103\n"
    "ds_read_b128 v[72:75], v103 offset:16\n"
    "v_mfma_f32_16x16x32_bf16 v[172:175], a[176:179], a[116:119], v[172:175]\n"
    "ds_read_b128 a[160:163], v51 offset:768\n"
    "s_waitcnt lgkmcnt(6)\n"
    "v_mfma_f32_16x16x32_bf16 v[196:199], a[104:107], a[180:183], v[196:199]\n"
    "buffer_load_dwordx4 a[60:63], v23, s[12:15], s59 offen sc1\n"
    "s_waitcnt lgkmcnt(5)\n"
    "ds_read_b128 v[76:79], v103 offset:144\n"
    "v_mfma_f32_16x16x32_bf16 v[196:199], a[120:123], a[184:187], v[196:199]\n"
    "ds_read_b32 v205, v102 offset:144\n"
    "v_mfma_f32_16x16x32_bf16 v[156:159], a[96:99], a[180:183], v[156:159]\n"
    "ds_read_b128 v[80:83], v103 offset:272\n"
    "v_mfma_f32_16x16x32_bf16 v[156:159], a[112:115], a[184:187], v[156:159]\n"
    "buffer_load_dwordx4 a[68:71], v23, s[12:15], s59 offen offset:2048 sc1\n"
    "s_add_u32 s59, s59, 0x3c00\n"
    "ds_read_b128 a[164:167], v48 offset:768\n"
    "v_mfma_f32_16x16x32_bf16 v[136:139], a[92:95], a[180:183], v[136:139]\n"
    "ds_read_b32 v207, v102 offset:272\n"
    "v_mfma_f32_16x16x32_bf16 v[136:139], a[108:111], a[184:187], v[136:139]\n"
    "ds_read_b128 v[84:87], v103 offset:400\n"
    "v_mfma_f32_16x16x32_bf16 v[176:179], a[180:183], a[100:103], v[176:179]\n"
    "ds_read_b32 v209, v102 offset:400\n"
    "v_mfma_f32_16x16x32_bf16 v[176:179], a[184:187], a[116:119], v[176:179]\n"
    "buffer_load_dwordx4 a[88:91], v24, s[12:15], s59 offen sc1\n"
    "ds_read_b128 a[168:171], v50 offset:768\n"
    "s_waitcnt lgkmcnt(12)\n"
    "v_mfma_f32_16x16x32_bf16 v[200:203], a[104:107], a[188:191], v[200:203]\n"
    "s_waitcnt lgkmcnt(11)\n"
    "ds_read_b128 v[88:91], v103 offset:528\n"
    "v_mfma_f32_16x16x32_bf16 v[200:203], a[120:123], a[192:195], v[200:203]\n"
    "ds_read_b32 v211, v102 offset:528\n"
    "v_mfma_f32_16x16x32_bf16 v[160:163], a[96:99], a[188:191], v[160:163]\n"
    "buffer_load_dwordx4 a[80:83], v23, s[12:15], s59 offen offset:1024 sc1\n"
    "ds_read_b32 v26, v102\n"
    "v_mfma_f32_16x16x32_bf16 v[160:163], a[112:115], a[192:195], v[160:163]\n"
    "ds_read_b128 a[172:175], v48 offset:21248\n"
    "v_mfma_f32_16x16x32_bf16 v[140:143], a[92:95], a[188:191], v[140:143]\n"
    "ds_read_b128 v[52:55], v103\n"
    "v_mfma_f32_16x16x32_bf16 v[140:143], a[108:111], a[192:195], v[140:143]\n"
    "s_mul_i32 s66, s41, 0x3800\n"
    "s_add_u32 m0, s66, 0x1a000\n"
    "s_add_u32 s66, m0, s62\n"
    "buffer_load_dwordx4 v7, s[12:15], s66 offen sc1 lds\n"
    "v_mfma_f32_16x16x32_bf16 v[180:183], a[188:191], a[100:103], v[180:183]\n"
    "ds_read_b32 v204, v102 offset:128\n"
    "v_mfma_f32_16x16x32_bf16 v[180:183], a[192:195], a[116:119], v[180:183]\n"
    "ds_read_b128 a[176:179], v50 offset:21248\n"
    "s_waitcnt lgkmcnt(15)\n"
    "v_mfma_f32_16x16x32_bf16 v[184:187], a[136:139], a[156:159], v[184:187]\n"
    "ds_read_b128 v[56:59], v103 offset:128\n"
    "v_mfma_f32_16x16x32_bf16 v[184:187], a[152:155], a[160:163], v[184:187]\n"
    "buffer_load_dwordx4 a[76:79], v23, s[12:15], s59 offen sc1\n"
    "ds_read_b32 v206, v102 offset:256\n"
    "v_mfma_f32_16x16x32_bf16 v[144:147], a[128:131], a[156:159], v[144:147]\n"
    "ds_read_b128 v[60:63], v103 offset:256\n"
    "v_mfma_f32_16x16x32_bf16 v[144:147], a[144:147], a[160:163], v[144:147]\n"
    "ds_read_b128 a[180:183], v48 offset:41728\n"
    "v_mfma_f32_16x16x32_bf16 v[12:15], a[124:127], a[0:3], v[12:15]\n"
    "ds_read_b32 v208, v102 offset:384\n"
    "v_mfma_f32_16x16x32_bf16 v[12:15], a[140:143], a[0:3], v[12:15]\n"
    "ds_read_b128 v[64:67], v103 offset:384\n"
    "v_mfma_f32_16x16x32_bf16 v[164:167], a[156:159], a[132:135], v[164:167]\n"
    "buffer_load_dwordx4 a[84:87], v23, s[12:15], s59 offen offset:2048 sc1\n"
    "s_add_u32 s59, s59, 0x3c00\n"
    "s_waitcnt vmcnt(17)\n"
    "v_mfma_f32_16x16x32_bf16 v[164:167], a[160:163], a[148:151], v[164:167]\n"
    "ds_read_b128 a[184:187], v50 offset:41728\n"
    "s_waitcnt lgkmcnt(15)\n"
    "v_mfma_f32_16x16x32_bf16 v[188:191], a[136:139], a[164:167], v[188:191]\n"
    "s_waitcnt lgkmcnt(14)\n"
    "ds_read_b32 v210, v102 offset:512\n"
    "v_mfma_f32_16x16x32_bf16 v[188:191], a[152:155], a[168:171], v[188:191]\n"
    "ds_read_b128 v[68:71], v103 offset:512\n"
    "v_cmp_eq_u32 vcc, 0, v104\n"
    "v_cmp_eq_u32 s[30:31], 0, v105\n"
    "v_mfma_f32_16x16x32_bf16 v[148:151], a[128:131], a[164:167], v[148:151]\n"
    "v_mul_f32 v205, s26, v205\n"
    "v_mul_f32 v207, s26, v207\n"
    "v_mfma_f32_16x16x32_bf16 v[148:151], a[144:147], a[168:171], v[148:151]\n"
    "buffer_load_dwordx4 v7, s[12:15], s66 offen offset:1024 sc1 lds\n"
    "ds_read_b128 a[188:191], v48 offset:62208\n"
    "v_cndmask_b32 v224, v72, v76, vcc\n"
    "v_mfma_f32_16x16x32_bf16 v[128:131], a[124:127], a[164:167], v[128:131]\n"
    "v_cndmask_b32 v225, v73, v77, s[30:31]\n"
    "v_cndmask_b32 v228, v76, v80, vcc\n"
    "v_mfma_f32_16x16x32_bf16 v[128:131], a[140:143], a[168:171], v[128:131]\n"
    "v_cndmask_b32 v229, v77, v81, s[30:31]\n"
    "v_cndmask_b32 v232, v80, v84, vcc\n"
    "v_mfma_f32_16x16x32_bf16 v[168:171], a[164:167], a[132:135], v[168:171]\n"
    "v_cndmask_b32 v233, v81, v85, s[30:31]\n"
    "s_waitcnt lgkmcnt(15)\n"
    "v_cndmask_b32 v236, v84, v88, vcc\n"
    "v_mfma_f32_16x16x32_bf16 v[168:171], a[168:171], a[148:151], v[168:171]\n"
    "ds_read_b128 a[192:195], v50 offset:62208\n"
    "s_waitcnt lgkmcnt(14)\n"
    "v_cndmask_b32 v237, v85, v89, s[30:31]\n"
    "v_mfma_f32_16x16x32_bf16 v[192:195], a[136:139], a[172:175], v[192:195]\n"
    "buffer_load_dwordx4 v7, s[12:15], s66 offen offset:2048 sc1 lds\n"
    "s_waitcnt lgkmcnt(11)\n"
    "v_cmp_eq_u32 vcc, 0, v106\n"
    "v_mfma_f32_16x16x32_bf16 v[192:195], a[152:155], a[176:179], v[192:195]\n"
    "v_cmp_eq_u32 s[30:31], 0, v107\n"
    "v_mul_f32 v209, s26, v209\n"
    "v_mfma_f32_16x16x32_bf16 v[152:155], a[128:131], a[172:175], v[152:155]\n"
    "v_mul_f32 v211, s26, v211\n"
    "v_cndmask_b32 v226, v74, v78, vcc\n"
    "v_mfma_f32_16x16x32_bf16 v[152:155], a[144:147], a[176:179], v[152:155]\n"
    "ds_read_b128 a[156:159], v45 offset:1024\n"
    "v_cndmask_b32 v227, v75, v79, s[30:31]\n"
    "v_cndmask_b32 v230, v78, v82, vcc\n"
    "v_mfma_f32_16x16x32_bf16 v[132:135], a[124:127], a[172:175], v[132:135]\n"
    "v_cndmask_b32 v231, v79, v83, s[30:31]\n"
    "v_cndmask_b32 v234, v82, v86, vcc\n"
    "v_mfma_f32_16x16x32_bf16 v[132:135], a[140:143], a[176:179], v[132:135]\n"
    "buffer_load_dwordx4 v7, s[12:15], s66 offen offset:3072 sc1 lds\n"
    "v_cndmask_b32 v235, v83, v87, s[30:31]\n"
    "v_cndmask_b32 v238, v86, v90, vcc\n"
    "v_mfma_f32_16x16x32_bf16 v[172:175], a[172:175], a[132:135], v[172:175]\n"
    "v_cndmask_b32 v239, v87, v91, s[30:31]\n"
    "v_mul_f32 v76, v224, v205\n"
    "v_mfma_f32_16x16x32_bf16 v[172:175], a[176:179], a[148:151], v[172:175]\n"
    "ds_read_b128 a[160:163], v47 offset:1024\n"
    "s_waitcnt lgkmcnt(9)\n"
    "v_mul_f32 v77, v225, v205\n"
    "v_mfma_f32_16x16x32_bf16 v[196:199], a[136:139], a[180:183], v[196:199]\n"
    "s_mul_i32 s66, s41, 0x3800\n"
    "s_add_u32 m0, s66, 0x1b000\n"
    "s_add_u32 s66, m0, s62\n"
    "buffer_load_dwordx4 v7, s[12:15], s66 offen sc1 lds\n"
    "s_waitcnt lgkmcnt(6)\n"
    "v_mfma_f32_16x16x32_bf16 v[196:199], a[152:155], a[184:187], v[196:199]\n"
    "v_mul_f32 v78, v226, v205\n"
    "v_mul_f32 v79, v227, v205\n"
    "v_mfma_f32_16x16x32_bf16 v[156:159], a[128:131], a[180:183], v[156:159]\n"
    "v_mul_f32 v80, v228, v207\n"
    "v_mul_f32 v81, v229, v207\n"
    "v_mfma_f32_16x16x32_bf16 v[156:159], a[144:147], a[184:187], v[156:159]\n"
    "buffer_load_dwordx4 v7, s[12:15], s66 offen offset:1024 sc1 lds\n"
    "ds_read_b128 a[164:167], v49 offset:1024\n"
    "v_mul_f32 v82, v230, v207\n"
    "v_mfma_f32_16x16x32_bf16 v[136:139], a[124:127], a[180:183], v[136:139]\n"
    "v_mul_f32 v83, v231, v207\n"
    "v_mul_f32 v84, v232, v209\n"
    "v_mfma_f32_16x16x32_bf16 v[136:139], a[140:143], a[184:187], v[136:139]\n"
    "v_mul_f32 v85, v233, v209\n"
    "v_mul_f32 v86, v234, v209\n"
    "v_mfma_f32_16x16x32_bf16 v[176:179], a[180:183], a[132:135], v[176:179]\n"
    "v_mul_f32 v87, v235, v209\n"
    "v_mul_f32 v88, v236, v211\n"
    "v_mfma_f32_16x16x32_bf16 v[176:179], a[184:187], a[148:151], v[176:179]\n"
    "buffer_load_dwordx4 v7, s[12:15], s66 offen offset:2048 sc1 lds\n"
    "ds_read_b128 a[168:171], v51 offset:1024\n"
    "s_waitcnt lgkmcnt(5)\n"
    "v_mfma_f32_16x16x32_bf16 v[200:203], a[136:139], a[188:191], v[200:203]\n"
    "s_waitcnt lgkmcnt(4)\n"
    "v_mul_f32 v89, v237, v211\n"
    "v_mul_f32 v90, v238, v211\n"
    "v_mfma_f32_16x16x32_bf16 v[200:203], a[152:155], a[192:195], v[200:203]\n"
    "v_mul_f32 v91, v239, v211\n"
    "v_mfma_f32_16x16x32_bf16 v[160:163], a[128:131], a[188:191], v[160:163]\n"
    "buffer_load_dwordx4 v7, s[12:15], s66 offen offset:3072 sc1 lds\n"
    "v_mfma_f32_16x16x32_bf16 v[160:163], a[144:147], a[192:195], v[160:163]\n"
    "ds_read_b128 a[172:175], v44 offset:1024\n"
    "v_mfma_f32_16x16x32_bf16 v[140:143], a[124:127], a[188:191], v[140:143]\n"
    "v_mfma_f32_16x16x32_bf16 v[140:143], a[140:143], a[192:195], v[140:143]\n"
    "s_mul_i32 s66, s41, 0x3800\n"
    "s_add_u32 m0, s66, 0x1c000\n"
    "s_add_u32 s66, m0, s62\n"
    "buffer_load_dwordx4 v7, s[12:15], s66 offen sc1 lds\n"
    "v_mfma_f32_16x16x32_bf16 v[180:183], a[188:191], a[132:135], v[180:183]\n"
    "v_mfma_f32_16x16x32_bf16 v[180:183], a[192:195], a[148:151], v[180:183]\n"
    "ds_read_b128 a[176:179], v46 offset:1024\n"
    "s_waitcnt vmcnt(24) lgkmcnt(5)\n"
    "v_mfma_f32_16x16x32_bf16 v[184:187], a[40:43], a[156:159], v[184:187]\n"
    "s_waitcnt vmcnt(20) lgkmcnt(4)\n"
    "v_mfma_f32_16x16x32_bf16 v[184:187], a[56:59], a[160:163], v[184:187]\n"
    "buffer_load_dwordx4 v7, s[12:15], s66 offen offset:1024 sc1 lds\n"
    "s_waitcnt vmcnt(17) lgkmcnt(3)\n"
    "v_mfma_f32_16x16x32_bf16 v[184:187], a[72:75], a[164:167], v[184:187]\n"
    "s_waitcnt vmcnt(13) lgkmcnt(2)\n"
    "v_mfma_f32_16x16x32_bf16 v[184:187], a[88:91], a[168:171], v[184:187]\n"
    "ds_read_b128 a[180:183], v48 offset:1024\n"
    "v_mfma_f32_16x16x32_bf16 v[144:147], a[32:35], a[156:159], v[144:147]\n"
    "v_mfma_f32_16x16x32_bf16 v[144:147], a[48:51], a[160:163], v[144:147]\n"
    "v_mfma_f32_16x16x32_bf16 v[144:147], a[64:67], a[164:167], v[144:147]\n"
    "buffer_load_dwordx4 v7, s[12:15], s66 offen offset:2048 sc1 lds\n"
    "s_waitcnt vmcnt(13)\n"
    "v_mfma_f32_16x16x32_bf16 v[144:147], a[80:83], a[168:171], v[144:147]\n"
    "ds_read_b128 a[184:187], v50 offset:1024\n"
    "v_mfma_f32_16x16x32_bf16 v[12:15], a[28:31], a[0:3], v[12:15]\n"
    "v_mfma_f32_16x16x32_bf16 v[12:15], a[44:47], a[0:3], v[12:15]\n"
    "v_mfma_f32_16x16x32_bf16 v[12:15], a[60:63], a[0:3], v[12:15]\n"
    "s_waitcnt vmcnt(11)\n"
    "v_mfma_f32_16x16x32_bf16 v[12:15], a[76:79], a[0:3], v[12:15]\n"
    "buffer_load_dwordx4 v7, s[12:15], s66 offen offset:3072 sc1 lds\n"
    "ds_read_b128 a[188:191], v44 offset:21504\n"
    "v_mfma_f32_16x16x32_bf16 v[164:167], a[156:159], a[36:39], v[164:167]\n"
    "v_mfma_f32_16x16x32_bf16 v[164:167], a[160:163], a[52:55], v[164:167]\n"
    "v_mfma_f32_16x16x32_bf16 v[164:167], a[164:167], a[68:71], v[164:167]\n"
    "s_waitcnt vmcnt(11)\n"
    "v_mfma_f32_16x16x32_bf16 v[164:167], a[168:171], a[84:87], v[164:167]\n"
    "ds_read_b128 a[192:195], v46 offset:21504\n"
    "s_waitcnt lgkmcnt(5)\n"
    "v_mfma_f32_16x16x32_bf16 v[188:191], a[40:43], a[172:175], v[188:191]\n"
    "s_mul_i32 s66, s41, 0x3800\n"
    "s_add_u32 m0, s66, 0x1d000\n"
    "s_add_u32 s66, m0, s62\n"
    "buffer_load_dwordx4 v7, s[12:15], s66 offen sc1 lds\n"
    "s_waitcnt lgkmcnt(4)\n"
    "v_mfma_f32_16x16x32_bf16 v[188:191], a[56:59], a[176:179], v[188:191]\n"
    "s_waitcnt lgkmcnt(3)\n"
    "v_mfma_f32_16x16x32_bf16 v[188:191], a[72:75], a[180:183], v[188:191]\n"
    "s_waitcnt lgkmcnt(2)\n"
    "v_mfma_f32_16x16x32_bf16 v[188:191], a[88:91], a[184:187], v[188:191]\n"
    "ds_read_b128 a[156:159], v48 offset:21504\n"
    "v_mov_b32_dpp v36, v184 row_newbcast:0 row_mask:0xf bank_mask:0xf\n"
    "v_mov_b32_dpp v37, v185 row_newbcast:0 row_mask:0xf bank_mask:0xf\n"
    "v_mfma_f32_16x16x32_bf16 v[148:151], a[32:35], a[172:175], v[148:151]\n"
    "v_mov_b32_dpp v38, v186 row_newbcast:0 row_mask:0xf bank_mask:0xf\n"
    "v_mov_b32_dpp v39, v187 row_newbcast:0 row_mask:0xf bank_mask:0xf\n"
    "v_mfma_f32_16x16x32_bf16 v[148:151], a[48:51], a[176:179], v[148:151]\n"
    "buffer_load_dwordx4 v7, s[12:15], s66 offen offset:1024 sc1 lds\n"
    "v_mov_b32_dpp v72, v144 row_newbcast:0 row_mask:0xf bank_mask:0xf\n"
    "v_mov_b32_dpp v73, v145 row_newbcast:0 row_mask:0xf bank_mask:0xf\n"
    "v_mfma_f32_16x16x32_bf16 v[148:151], a[64:67], a[180:183], v[148:151]\n"
    "v_mov_b32_dpp v74, v146 row_newbcast:0 row_mask:0xf bank_mask:0xf\n"
    "v_mov_b32_dpp v75, v147 row_newbcast:0 row_mask:0xf bank_mask:0xf\n"
    "v_mfma_f32_16x16x32_bf16 v[148:151], a[80:83], a[184:187], v[148:151]\n"
    "ds_read_b128 a[160:163], v50 offset:21504\n"
    "v_mov_b32_dpp v12, v12 row_newbcast:0 row_mask:0xf bank_mask:0xf\n"
    "v_mov_b32_dpp v13, v13 row_newbcast:0 row_mask:0xf bank_mask:0xf\n"
    "v_mfma_f32_16x16x32_bf16 v[128:131], a[28:31], a[172:175], v[128:131]\n"
    "buffer_load_dwordx4 a[252:255], v7, s[12:15], s78 offen sc1\n"
    "v_mov_b32 v32, v164\n"
    "v_mov_b32_e64 v33, v164\n"
    "v_mfma_f32_16x16x32_bf16 v[128:131], a[44:47], a[176:179], v[128:131]\n"
    "v_mov_b32_dpp v14, v14 row_newbcast:0 row_mask:0xf bank_mask:0xf\n"
    "v_mov_b32_dpp v15, v15 row_newbcast:0 row_mask:0xf bank_mask:0xf\n"
    "v_mfma_f32_16x16x32_bf16 v[128:131], a[60:63], a[180:183], v[128:131]\n"
    "v_permlane32_swap_b32 v32, v33\n"
    "v_mfma_f32_16x16x32_bf16 v[128:131], a[76:79], a[184:187], v[128:131]\n"
    "buffer_load_dwordx4 a[248:251], v7, s[12:15], s78 offen offset:1024 sc1\n"
    "ds_read_b128 a[164:167], v44 offset:41984\n"
    "v_fma_f32 v144, v72, v26, v144\n"
    "v_mfma_f32_16x16x32_bf16 v[168:171], a[172:175], a[36:39], v[168:171]\n"
    "v_fma_f32 v145, v73, v26, v145\n"
    "v_fma_f32 v146, v74, v26, v146\n"
    "v_mfma_f32_16x16x32_bf16 v[168:171], a[176:179], a[52:55], v[168:171]\n"
    "v_fma_f32 v147, v75, v26, v147\n"
    "v_cvt_pk_bf16_f32 v144, v144, v145\n"
    "v_mfma_f32_16x16x32_bf16 v[168:171], a[180:183], a[68:71], v[168:171]\n"
    "v_cvt_pk_bf16_f32 v145, v146, v147\n"
    "v_fma_f32 v184, v36, v26, v184\n"
    "v_mfma_f32_16x16x32_bf16 v[168:171], a[184:187], a[84:87], v[168:171]\n"
    "buffer_load_dwordx4 a[244:247], v7, s[12:15], s78 offen offset:2048 sc1\n"
    "ds_read_b128 a[168:171], v46 offset:41984\n"
    "s_waitcnt lgkmcnt(5)\n"
    "v_mfma_f32_16x16x32_bf16 v[192:195], a[40:43], a[188:191], v[192:195]\n"
    "s_waitcnt lgkmcnt(4)\n"
    "v_fma_f32 v185, v37, v26, v185\n"
    "v_fma_f32 v186, v38, v26, v186\n"
    "v_mfma_f32_16x16x32_bf16 v[192:195], a[56:59], a[192:195], v[192:195]\n"
    "s_waitcnt lgkmcnt(3)\n"
    "v_fma_f32 v187, v39, v26, v187\n"
    "v_cvt_pk_bf16_f32 v146, v184, v185\n"
    "v_mfma_f32_16x16x32_bf16 v[192:195], a[72:75], a[156:159], v[192:195]\n"
    "buffer_load_dwordx4 a[240:243], v7, s[12:15], s78 offen offset:3072 sc1\n"
    "s_add_u32 s78, s78, 0x1000\n"
    "s_waitcnt lgkmcnt(2)\n"
    "v_mfma_f32_16x16x32_bf16 v[192:195], a[88:91], a[160:163], v[192:195]\n"
    "ds_read_b128 a[172:175], v48 offset:41984\n"
    "v_cvt_pk_bf16_f32 v147, v186, v187\n"
    "v_mfma_f32_16x16x32_bf16 v[152:155], a[32:35], a[188:191], v[152:155]\n"
    "ds_write_b64 v123, v[146:147]\n"
    "v_fma_f32 v164, v52, v32, v164\n"
    "v_fma_f32 v165, v53, v32, v165\n"
    "v_mfma_f32_16x16x32_bf16 v[152:155], a[48:51], a[192:195], v[152:155]\n"
    "buffer_load_dwordx4 a[236:239], v7, s[12:15], s78 offen sc1\n"
    "v_fma_f32 v166, v54, v32, v166\n"
    "v_fma_f32 v167, v55, v32, v167\n"
    "v_mfma_f32_16x16x32_bf16 v[152:155], a[64:67], a[156:159], v[152:155]\n"
    "v_cvt_pk_bf16_f32 v92, v164, v165\n"
    "v_cvt_pk_bf16_f32 v93, v166, v167\n"
    "v_mfma_f32_16x16x32_bf16 v[152:155], a[80:83], a[160:163], v[152:155]\n"
    "ds_read_b128 a[176:179], v50 offset:41984\n"
    "v_mfma_f32_16x16x32_bf16 v[132:135], a[28:31], a[188:191], v[132:135]\n"
    "ds_read_b64_tr_b16 a[196:197], v125 offset:256\n"
    "v_fma_f32 v148, v72, v204, v148\n"
    "v_fma_f32 v149, v73, v204, v149\n"
    "v_mfma_f32_16x16x32_bf16 v[132:135], a[44:47], a[192:195], v[132:135]\n"
    "buffer_load_dwordx4 a[232:235], v7, s[12:15], s78 offen offset:1024 sc1\n"
    "v_fma_f32 v150, v74, v204, v150\n"
    "v_fma_f32 v151, v75, v204, v151\n"
    "v_mfma_f32_16x16x32_bf16 v[132:135], a[60:63], a[156:159], v[132:135]\n"
    "v_cvt_pk_bf16_f32 v148, v148, v149\n"
    "v_cvt_pk_bf16_f32 v149, v150, v151\n"
    "v_mfma_f32_16x16x32_bf16 v[132:135], a[76:79], a[160:163], v[132:135]\n"
    "ds_read_b128 a[180:183], v44 offset:62464\n"
    "v_fma_f32 v188, v36, v204, v188\n"
    "v_fma_f32 v189, v37, v204, v189\n"
    "v_mfma_f32_16x16x32_bf16 v[172:175], a[188:191], a[36:39], v[172:175]\n"
    "v_fma_f32 v190, v38, v204, v190\n"
    "v_fma_f32 v191, v39, v204, v191\n"
    "v_mfma_f32_16x16x32_bf16 v[172:175], a[192:195], a[52:55], v[172:175]\n"
    "v_cvt_pk_bf16_f32 v150, v188, v189\n"
    "v_cvt_pk_bf16_f32 v151, v190, v191\n"
    "v_mfma_f32_16x16x32_bf16 v[172:175], a[156:159], a[68:71], v[172:175]\n"
    "buffer_load_dwordx4 a[228:231], v7, s[12:15], s78 offen offset:2048 sc1\n"
    "ds_write_b64 v123, v[150:151] offset:512\n"
    "v_fma_f32 v128, v12, v204, v128\n"
    "v_mfma_f32_16x16x32_bf16 v[172:175], a[160:163], a[84:87], v[172:175]\n"
    "ds_read_b128 a[184:187], v46 offset:62464\n"
    "s_waitcnt lgkmcnt(8)\n"
    "v_fma_f32 v129, v13, v204, v129\n"
    "v_mfma_f32_16x16x32_bf16 v[196:199], a[40:43], a[164:167], v[196:199]\n"
    "s_waitcnt lgkmcnt(7)\n"
    "v_fma_f32 v130, v14, v204, v130\n"
    "v_fma_f32 v131, v15, v204, v131\n"
    "v_mfma_f32_16x16x32_bf16 v[196:199], a[56:59], a[168:171], v[196:199]\n"
    "s_waitcnt lgkmcnt(6)\n"
    "v_cvt_pk_bf16_f32 v128, v128, v129\n"
    "v_cvt_pk_bf16_f32 v129, v130, v131\n"
    "v_mfma_f32_16x16x32_bf16 v[196:199], a[72:75], a[172:175], v[196:199]\n"
    "s_waitcnt lgkmcnt(4)\n"
    "ds_read_b64 v[130:131], v124 offset:512\n"
    "v_fma_f32 v168, v56, v32, v168\n"
    "v_mfma_f32_16x16x32_bf16 v[196:199], a[88:91], a[176:179], v[196:199]\n"
    "buffer_load_dwordx4 a[224:227], v7, s[12:15], s78 offen offset:3072 sc1\n"
    "s_add_u32 s78, s78, 0x1000\n"
    "ds_read_b128 a[188:191], v48 offset:62464\n"
    "v_mfma_f32_16x16x32_bf16 v[156:159], a[32:35], a[164:167], v[156:159]\n"
    "v_fma_f32 v169, v57, v32, v169\n"
    "v_fma_f32 v170, v58, v32, v170\n"
    "v_mfma_f32_16x16x32_bf16 v[156:159], a[48:51], a[168:171], v[156:159]\n"
    "v_fma_f32 v171, v59, v32, v171\n"
    "v_cvt_pk_bf16_f32 v94, v168, v169\n"
    "v_mfma_f32_16x16x32_bf16 v[156:159], a[64:67], a[172:175], v[156:159]\n"
    "v_cvt_pk_bf16_f32 v95, v170, v171\n"
    "ds_read_b64_tr_b16 a[198:199], v125 offset:768\n"
    "v_mfma_f32_16x16x32_bf16 v[156:159], a[80:83], a[176:179], v[156:159]\n"
    "ds_read_b128 a[192:195], v50 offset:62464\n"
    "v_fma_f32 v152, v72, v206, v152\n"
    "v_fma_f32 v153, v73, v206, v153\n"
    "v_mfma_f32_16x16x32_bf16 v[136:139], a[28:31], a[164:167], v[136:139]\n"
    "buffer_load_dwordx4 a[220:223], v7, s[12:15], s78 offen sc1\n"
    "v_fma_f32 v154, v74, v206, v154\n"
    "v_fma_f32 v155, v75, v206, v155\n"
    "v_mfma_f32_16x16x32_bf16 v[136:139], a[44:47], a[168:171], v[136:139]\n"
    "v_cvt_pk_bf16_f32 v152, v152, v153\n"
    "v_cvt_pk_bf16_f32 v153, v154, v155\n"
    "v_mfma_f32_16x16x32_bf16 v[136:139], a[60:63], a[172:175], v[136:139]\n"
    "v_fma_f32 v192, v36, v206, v192\n"
    "v_fma_f32 v193, v37, v206, v193\n"
    "v_mfma_f32_16x16x32_bf16 v[136:139], a[76:79], a[176:179], v[136:139]\n"
    "v_fma_f32 v194, v38, v206, v194\n"
    "v_fma_f32 v195, v39, v206, v195\n"
    "v_mfma_f32_16x16x32_bf16 v[176:179], a[164:167], a[36:39], v[176:179]\n"
    "v_cvt_pk_bf16_f32 v154, v192, v193\n"
    "v_cvt_pk_bf16_f32 v155, v194, v195\n"
    "v_mfma_f32_16x16x32_bf16 v[176:179], a[168:171], a[52:55], v[176:179]\n"
    "buffer_load_dwordx4 a[216:219], v7, s[12:15], s78 offen offset:1024 sc1\n"
    "ds_write_b64 v123, v[154:155] offset:1024\n"
    "v_fma_f32 v132, v12, v206, v132\n"
    "v_mfma_f32_16x16x32_bf16 v[176:179], a[172:175], a[68:71], v[176:179]\n"
    "v_fma_f32 v133, v13, v206, v133\n"
    "v_fma_f32 v134, v14, v206, v134\n"
    "v_mfma_f32_16x16x32_bf16 v[176:179], a[176:179], a[84:87], v[176:179]\n"
    "s_waitcnt lgkmcnt(7)\n"
    "v_fma_f32 v135, v15, v206, v135\n"
    "v_cvt_pk_bf16_f32 v132, v132, v133\n"
    "v_mfma_f32_16x16x32_bf16 v[200:203], a[40:43], a[180:183], v[200:203]\n"
    "buffer_load_dwordx4 a[212:215], v7, s[12:15], s78 offen offset:2048 sc1\n"
    "s_waitcnt lgkmcnt(5)\n"
    "v_cvt_pk_bf16_f32 v133, v134, v135\n"
    "v_mfma_f32_16x16x32_bf16 v[200:203], a[56:59], a[184:187], v[200:203]\n"
    "s_waitcnt lgkmcnt(3)\n"
    "ds_read_b64 v[134:135], v124 offset:1024\n"
    "v_fma_f32 v172, v60, v32, v172\n"
    "v_mfma_f32_16x16x32_bf16 v[200:203], a[72:75], a[188:191], v[200:203]\n"
    "s_waitcnt lgkmcnt(2)\n"
    "v_fma_f32 v173, v61, v32, v173\n"
    "v_fma_f32 v174, v62, v32, v174\n"
    "v_mfma_f32_16x16x32_bf16 v[200:203], a[88:91], a[192:195], v[200:203]\n"
    "buffer_load_dwordx4 a[208:211], v7, s[12:15], s78 offen offset:3072 sc1\n"
    "s_add_u32 s78, s78, 0x1000\n"
    "v_fma_f32 v175, v63, v32, v175\n"
    "v_mfma_f32_16x16x32_bf16 v[160:163], a[32:35], a[180:183], v[160:163]\n"
    "v_cvt_pk_bf16_f32 v96, v172, v173\n"
    "v_cvt_pk_bf16_f32 v97, v174, v175\n"
    "v_mfma_f32_16x16x32_bf16 v[160:163], a[48:51], a[184:187], v[160:163]\n"
    "ds_read_b64_tr_b16 a[200:201], v125 offset:1280\n"
    "v_fma_f32 v156, v72, v208, v156\n"
    "v_fma_f32 v157, v73, v208, v157\n"
    "v_mfma_f32_16x16x32_bf16 v[160:163], a[64:67], a[188:191], v[160:163]\n"
    "v_fma_f32 v158, v74, v208, v158\n"
    "v_fma_f32 v159, v75, v208, v159\n"
    "v_mfma_f32_16x16x32_bf16 v[160:163], a[80:83], a[192:195], v[160:163]\n"
    "buffer_load_dwordx4 a[24:27], v7, s[12:15], s78 offen sc1\n"
    "v_cvt_pk_bf16_f32 v156, v156, v157\n"
    "v_cvt_pk_bf16_f32 v157, v158, v159\n"
    "v_mfma_f32_16x16x32_bf16 v[140:143], a[28:31], a[180:183], v[140:143]\n"
    "v_fma_f32 v196, v36, v208, v196\n"
    "v_fma_f32 v197, v37, v208, v197\n"
    "v_mfma_f32_16x16x32_bf16 v[140:143], a[44:47], a[184:187], v[140:143]\n"
    "v_fma_f32 v198, v38, v208, v198\n"
    "v_fma_f32 v199, v39, v208, v199\n"
    "v_mfma_f32_16x16x32_bf16 v[140:143], a[60:63], a[188:191], v[140:143]\n"
    "buffer_load_dwordx4 a[20:23], v7, s[12:15], s78 offen offset:1024 sc1\n"
    "v_cvt_pk_bf16_f32 v158, v196, v197\n"
    "v_cvt_pk_bf16_f32 v159, v198, v199\n"
    "v_mfma_f32_16x16x32_bf16 v[140:143], a[76:79], a[192:195], v[140:143]\n"
    "ds_write_b64 v123, v[158:159] offset:1536\n"
    "v_fma_f32 v136, v12, v208, v136\n"
    "v_fma_f32 v137, v13, v208, v137\n"
    "v_mfma_f32_16x16x32_bf16 v[180:183], a[180:183], a[36:39], v[180:183]\n"
    "v_fma_f32 v138, v14, v208, v138\n"
    "v_fma_f32 v139, v15, v208, v139\n"
    "v_mfma_f32_16x16x32_bf16 v[180:183], a[184:187], a[52:55], v[180:183]\n"
    "buffer_load_dwordx4 a[16:19], v7, s[12:15], s78 offen offset:2048 sc1\n"
    "v_cvt_pk_bf16_f32 v136, v136, v137\n"
    "v_cvt_pk_bf16_f32 v137, v138, v139\n"
    "v_mfma_f32_16x16x32_bf16 v[180:183], a[188:191], a[68:71], v[180:183]\n"
    "ds_read_b64 v[138:139], v124 offset:1536\n"
    "v_fma_f32 v176, v64, v32, v176\n"
    "v_fma_f32 v177, v65, v32, v177\n"
    "v_mfma_f32_16x16x32_bf16 v[180:183], a[192:195], a[84:87], v[180:183]\n"
    "v_fma_f32 v178, v66, v32, v178\n"
    "v_fma_f32 v179, v67, v32, v179\n"
    "s_barrier\n"
    "v_cvt_pk_bf16_f32 v98, v176, v177\n"
    "v_cvt_pk_bf16_f32 v99, v178, v179\n"
    "ds_read_b64_tr_b16 a[202:203], v125 offset:1792\n"
    "v_pk_fma_f32 v[160:161], v[72:73], v[210:211], v[160:161] op_sel_hi:[1,0,1]\n"
    "v_pk_fma_f32 v[162:163], v[74:75], v[210:211], v[162:163] op_sel_hi:[1,0,1]\n"
    "v_cvt_pk_bf16_f32 v160, v160, v161\n"
    "v_cvt_pk_bf16_f32 v161, v162, v163\n"
    "v_pk_fma_f32 v[200:201], v[36:37], v[210:211], v[200:201] op_sel_hi:[1,0,1]\n"
    "v_pk_fma_f32 v[202:203], v[38:39], v[210:211], v[202:203] op_sel_hi:[1,0,1]\n"
    "v_cvt_pk_bf16_f32 v162, v200, v201\n"
    "v_cvt_pk_bf16_f32 v163, v202, v203\n"
    "ds_write_b64 v123, v[162:163] offset:2048\n"
    "v_pk_fma_f32 v[140:141], v[12:13], v[210:211], v[140:141] op_sel_hi:[1,0,1]\n"
    "v_pk_fma_f32 v[142:143], v[14:15], v[210:211], v[142:143] op_sel_hi:[1,0,1]\n"
    "v_cvt_pk_bf16_f32 v140, v140, v141\n"
    "v_cvt_pk_bf16_f32 v141, v142, v143\n"
    "ds_read_b64 v[142:143], v124 offset:2048\n"
    "v_pk_fma_f32 v[180:181], v[68:69], v[32:33], v[180:181] op_sel_hi:[1,0,1]\n"
    "s_mul_i32 s66, s41, 0x2000\n"
    "s_add_u32 m0, s66, 0xf000\n"
    "s_add_u32 s66, m0, s62\n"
    "buffer_load_dwordx4 v7, s[12:15], s66 offen sc1 lds\n"
    "v_pk_fma_f32 v[182:183], v[70:71], v[32:33], v[182:183] op_sel_hi:[1,0,1]\n"
    "v_cvt_pk_bf16_f32 v100, v180, v181\n"
    "v_cvt_pk_bf16_f32 v101, v182, v183\n"
    "ds_read_b64_tr_b16 a[204:205], v125 offset:2304\n"
    "v_mfma_f32_16x16x32_bf16 v[188:191], v[144:147], v[128:131], v[112:115]\n"
    "v_mfma_f32_16x16x32_bf16 v[56:59], v[148:151], v[128:131], v[104:107]\n"
    "s_waitcnt lgkmcnt(7)\n"
    "v_mfma_f32_16x16x32_bf16 v[192:195], v[148:151], v[132:135], v[108:111]\n"
    "v_mfma_f32_16x16x32_bf16 v[60:63], v[152:155], v[132:135], v[104:107]\n"
    "buffer_load_dwordx4 v7, s[12:15], s66 offen offset:1024 sc1 lds\n"
    "s_waitcnt lgkmcnt(4)\n"
    "v_mfma_f32_16x16x32_bf16 v[196:199], v[152:155], v[136:139], v[108:111]\n"
    "v_mfma_f32_16x16x32_bf16 v[64:67], v[156:159], v[136:139], v[104:107]\n"
    "s_waitcnt lgkmcnt(1)\n"
    "v_mfma_f32_16x16x32_bf16 v[200:203], v[156:159], v[140:143], v[108:111]\n"
    "v_max_f32 v56, v56, v188\n"
    "buffer_load_dwordx4 v7, s[12:15], s66 offen offset:2048 sc1 lds\n"
    "v_mfma_f32_16x16x32_bf16 v[68:71], v[160:163], v[140:143], v[104:107]\n"
    "v_max_f32 v57, v57, v189\n"
    "v_max_f32 v58, v58, v190\n"
    "v_max_f32 v59, v59, v191\n"
    "v_max_f32 v60, v60, v192\n"
    "v_max_f32 v61, v61, v193\n"
    "v_max_f32 v62, v62, v194\n"
    "v_max_f32 v63, v63, v195\n"
    "v_max_f32 v64, v64, v196\n"
    "v_max_f32 v65, v65, v197\n"
    "v_max_f32 v66, v66, v198\n"
    "v_max_f32 v67, v67, v199\n"
    "v_max_f32 v68, v68, v200\n"
    "buffer_load_dwordx4 v7, s[12:15], s66 offen offset:3072 sc1 lds\n"
    "v_max_f32 v69, v69, v201\n"
    "v_max_f32 v70, v70, v202\n"
    "v_max_f32 v71, v71, v203\n"
    "s_waitcnt lgkmcnt(0)\n"
    "v_pk_mul_f32 v[56:57], v[56:57], v[76:77]\n"
    "v_pk_mul_f32 v[58:59], v[58:59], v[78:79]\n"
    "v_max3_f32 v204, v56, v57, v58\n"
    "v_max_f32_e64 v204, v204, v59\n"
    "v_pk_mul_f32 v[60:61], v[60:61], v[80:81]\n"
    "v_pk_mul_f32 v[62:63], v[62:63], v[82:83]\n"
    "v_max3_f32 v206, v60, v61, v62\n"
    "v_max_f32_e64 v206, v206, v63\n"
    "v_pk_mul_f32 v[64:65], v[64:65], v[84:85]\n"
    "v_pk_mul_f32 v[66:67], v[66:67], v[86:87]\n"
    "v_max3_f32 v208, v64, v65, v66\n"
    "v_max_f32_e64 v208, v208, v67\n"
    "v_pk_mul_f32 v[68:69], v[68:69], v[88:89]\n"
    "s_mul_i32 s66, s41, 0x2000\n"
    "s_add_u32 m0, s66, 0x10000\n"
    "s_add_u32 s66, m0, s62\n"
    "buffer_load_dwordx4 v7, s[12:15], s66 offen sc1 lds\n"
    "v_pk_mul_f32 v[70:71], v[70:71], v[90:91]\n"
    "v_max3_f32 v210, v68, v69, v70\n"
    "v_max_f32 v210, v210, v71\n"
    "v_permlane32_swap_b32 v204, v206\n"
    "s_nop 0\n"
    "v_permlane32_swap_b32 v208, v210\n"
    "v_max_f32 v204, v204, v206\n"
    "v_max_f32 v208, v208, v210\n"
    "s_nop 1\n"
    "v_permlane16_swap_b32 v204, v208\n"
    "v_max_f32 v204, v204, v208\n"
    "v_mov_b32 v208, v204\n"
    "s_nop 1\n"
    "v_permlane16_swap_b32 v204, v208\n"
    "v_mov_b32 v206, v204\n"
    "buffer_load_dwordx4 v7, s[12:15], s66 offen offset:1024 sc1 lds\n"
    "v_mov_b32 v210, v208\n"
    "v_permlane32_swap_b32 v204, v206\n"
    "s_nop 0\n"
    "v_permlane32_swap_b32 v208, v210\n"
    "v_pk_add_f32 v[56:57], v[56:57], v[204:205] op_sel_hi:[1,0] neg_lo:[0,1] neg_hi:[0,1]\n"
    "v_pk_add_f32 v[58:59], v[58:59], v[204:205] op_sel_hi:[1,0] neg_lo:[0,1] neg_hi:[0,1]\n"
    "v_pk_add_f32 v[60:61], v[60:61], v[206:207] op_sel_hi:[1,0] neg_lo:[0,1] neg_hi:[0,1]\n"
    "v_pk_add_f32 v[62:63], v[62:63], v[206:207] op_sel_hi:[1,0] neg_lo:[0,1] neg_hi:[0,1]\n"
    "v_pk_add_f32 v[64:65], v[64:65], v[208:209] op_sel_hi:[1,0] neg_lo:[0,1] neg_hi:[0,1]\n"
    "v_pk_add_f32 v[66:67], v[66:67], v[208:209] op_sel_hi:[1,0] neg_lo:[0,1] neg_hi:[0,1]\n"
    "v_pk_add_f32 v[68:69], v[68:69], v[210:211] op_sel_hi:[1,0] neg_lo:[0,1] neg_hi:[0,1]\n"
    "v_pk_add_f32 v[70:71], v[70:71], v[210:211] op_sel_hi:[1,0] neg_lo:[0,1] neg_hi:[0,1]\n"
    "v_exp_f32 v56, v56\n"
    "v_exp_f32 v57, v57\n"
    "buffer_load_dwordx4 v7, s[12:15], s66 offen offset:2048 sc1 lds\n"
    "v_exp_f32 v58, v58\n"
    "v_exp_f32 v59, v59\n"
    "v_exp_f32 v60, v60\n"
    "v_exp_f32 v61, v61\n"
    "v_exp_f32 v62, v62\n"
    "v_exp_f32 v63, v63\n"
    "v_exp_f32 v64, v64\n"
    "v_exp_f32 v65, v65\n"
    "buffer_load_dwordx4 v7, s[12:15], s66 offen offset:3072 sc1 lds\n"
    "v_exp_f32 v66, v66\n"
    "v_exp_f32 v67, v67\n"
    "v_exp_f32 v68, v68\n"
    "v_exp_f32 v69, v69\n"
    "v_exp_f32 v70, v70\n"
    "v_exp_f32 v71, v71\n"
    "v_pk_add_f32 v[204:205], v[56:57], v[58:59]\n"
    "v_pk_add_f32 v[206:207], v[60:61], v[62:63]\n"
    "v_pk_add_f32 v[208:209], v[64:65], v[66:67]\n"
    "s_mul_i32 s66, s41, 0xc00\n"
    "s_add_u32 m0, s66, 0x17000\n"
    "s_add_u32 s66, m0, s62\n"
    "buffer_load_dwordx4 v7, s[12:15], s66 offen sc1 lds\n"
    "v_pk_add_f32 v[210:211], v[68:69], v[70:71]\n"
    "v_add_f32 v204, v204, v205\n"
    "v_add_f32 v206, v206, v207\n"
    "v_add_f32 v208, v208, v209\n"
    "v_add_f32 v210, v210, v211\n"
    "v_pk_mul_f32 v[56:57], v[56:57], v[224:225]\n"
    "v_pk_mul_f32 v[58:59], v[58:59], v[226:227]\n"
    "v_permlane32_swap_b32 v204, v206\n"
    "v_pk_mul_f32 v[60:61], v[60:61], v[228:229]\n"
    "v_permlane32_swap_b32 v208, v210\n"
    "v_add_f32 v204, v204, v206\n"
    "v_add_f32_e64 v208, v208, v210\n"
    "v_pk_mul_f32 v[62:63], v[62:63], v[230:231]\n"
    "v_pk_mul_f32 v[64:65], v[64:65], v[232:233]\n"
    "buffer_load_dwordx4 v7, s[12:15], s66 offen offset:1024 sc1 lds\n"
    "v_permlane16_swap_b32 v204, v208\n"
    "v_add_f32 v204, v204, v208\n"
    "v_rcp_f32 v204, v204\n"
    "v_pk_mul_f32 v[66:67], v[66:67], v[234:235]\n"
    "v_mov_b32 v208, v204\n"
    "v_pk_mul_f32 v[68:69], v[68:69], v[236:237]\n"
    "v_pk_mul_f32 v[70:71], v[70:71], v[238:239]\n"
    "v_permlane16_swap_b32 v204, v208\n"
    "v_mov_b32 v206, v204\n"
    "v_mov_b32 v210, v208\n"
    "s_nop 0\n"
    "v_permlane32_swap_b32 v204, v206\n"
    "v_permlane32_swap_b32 v208, v210\n"
    "buffer_load_dwordx4 v7, s[12:15], s66 offen offset:2048 sc1 lds\n"
    "v_pk_mul_f32 v[56:57], v[56:57], v[204:205] op_sel_hi:[1,0]\n"
    "v_pk_mul_f32 v[58:59], v[58:59], v[204:205] op_sel_hi:[1,0]\n"
    "v_pk_mul_f32 v[60:61], v[60:61], v[206:207] op_sel_hi:[1,0]\n"
    "v_pk_mul_f32 v[62:63], v[62:63], v[206:207] op_sel_hi:[1,0]\n"
    "v_pk_mul_f32 v[64:65], v[64:65], v[208:209] op_sel_hi:[1,0]\n"
    "v_pk_mul_f32 v[66:67], v[66:67], v[208:209] op_sel_hi:[1,0]\n"
    "v_pk_mul_f32 v[68:69], v[68:69], v[210:211] op_sel_hi:[1,0]\n"
    "v_pk_mul_f32 v[70:71], v[70:71], v[210:211] op_sel_hi:[1,0]\n"
    "v_cvt_pk_bf16_f32 v56, v56, v57\n"
    "v_cvt_pk_bf16_f32 v57, v58, v59\n"
    "v_and_b32 v128, v56, v116\n"
    "v_and_b32 v129, v57, v117\n"
    "v_and_b32 v130, v56, v118\n"
    "v_and_b32 v131, v57, v119\n"
    "v_cvt_pk_bf16_f32 v60, v60, v61\n"
    "s_mul_i32 s66, s41, 0x3c00\n"
    "s_add_u32 m0, s66, 0\n"
    "s_add_u32 s66, m0, s65\n"
    "buffer_load_dwordx4 v7, s[12:15], s66 offen sc1 lds\n"
    "v_cvt_pk_bf16_f32 v61, v62, v63\n"
    "v_mfma_f32_16x16x32_bf16 v[188:191], v[92:95], v[128:131], 0\n"
    "v_and_b32 v132, v60, v116\n"
    "v_and_b32 v133, v61, v117\n"
    "v_mfma_f32_16x16x32_bf16 v[168:171], a[196:199], v[128:131], 0\n"
    "v_and_b32 v134, v60, v118\n"
    "v_and_b32 v135, v61, v119\n"
    "v_cvt_pk_bf16_f32 v64, v64, v65\n"
    "v_cvt_pk_bf16_f32 v65, v66, v67\n"
    "v_mfma_f32_16x16x32_bf16 v[192:195], v[94:97], v[132:135], 0\n"
    "buffer_load_dwordx4 v7, s[12:15], s66 offen offset:1024 sc1 lds\n"
    "v_and_b32 v136, v64, v116\n"
    "v_and_b32 v137, v65, v117\n"
    "v_mfma_f32_16x16x32_bf16 v[172:175], a[198:201], v[132:135], 0\n"
    "v_and_b32 v138, v64, v118\n"
    "v_and_b32 v139, v65, v119\n"
    "v_cvt_pk_bf16_f32 v68, v68, v69\n"
    "v_cvt_pk_bf16_f32 v69, v70, v71\n"
    "v_mfma_f32_16x16x32_bf16 v[196:199], v[96:99], v[136:139], 0\n"
    "buffer_load_dwordx4 v7, s[12:15], s66 offen offset:2048 sc1 lds\n"
    "v_and_b32 v140, v68, v116\n"
    "v_and_b32 v141, v69, v117\n"
    "v_mfma_f32_16x16x32_bf16 v[176:179], a[200:203], v[136:139], 0\n"
    "v_and_b32 v142, v68, v118\n"
    "v_and_b32 v143, v69, v119\n"
    "s_mov_b32 vcc_lo, 0xffff0000\n"
    "s_mov_b32 vcc_hi, 0xffff0000\n"
    "v_mfma_f32_16x16x32_bf16 v[200:203], v[98:101], v[140:143], 0\n"
    "v_mfma_f32_16x16x32_bf16 v[180:183], a[202:205], v[140:143], 0\n"
    "buffer_load_dwordx4 v7, s[12:15], s66 offen offset:3072 sc1 lds\n"
    "s_mov_b32 s24, 0\n"
    "s_mov_b32 s25, -1\n"
    "v_cvt_pk_bf16_f32 v144, v188, v189\n"
    "v_cvt_pk_bf16_f32 v145, v190, v191\n"
    "v_cvt_pk_bf16_f32 v146, v192, v193\n"
    "v_cvt_pk_bf16_f32 v147, v194, v195\n"
    "v_cvt_pk_bf16_f32 v152, v168, v169\n"
    "v_cvt_pk_bf16_f32 v153, v170, v171\n"
    "v_cvt_pk_bf16_f32 v154, v172, v173\n"
    "v_cvt_pk_bf16_f32 v155, v174, v175\n"
    "v_permlane16_swap_b32 v144, v146\n"
    "v_permlane16_swap_b32 v145, v147\n"
    "buffer_store_dwordx4 v[144:147], v126, s[12:15], s67 offen sc1\n"
    "v_cvt_pk_bf16_f32 v148, v196, v197\n"
    "v_cvt_pk_bf16_f32 v149, v198, v199\n"
    "v_cvt_pk_bf16_f32 v150, v200, v201\n"
    "v_cvt_pk_bf16_f32 v151, v202, v203\n"
    "v_cvt_pk_bf16_f32 v156, v176, v177\n"
    "v_cvt_pk_bf16_f32 v157, v178, v179\n"
    "v_cvt_pk_bf16_f32 v158, v180, v181\n"
    "v_cvt_pk_bf16_f32 v159, v182, v183\n"
    "v_permlane16_swap_b32 v148, v150\n"
    "v_permlane16_swap_b32 v149, v151\n"
    "buffer_store_dwordx4 v[148:151], v126, s[12:15], s68 offen sc1\n"
    "v_cndmask_b32 v152, v152, v154, vcc\n"
    "v_cndmask_b32 v156, v156, v158, vcc\n"
    "v_cndmask_b32 v153, v153, v155, vcc\n"
    "v_cndmask_b32 v157, v157, v159, vcc\n"
    "v_cndmask_b32 v152, v152, v156, s[24:25]\n"
    "v_cndmask_b32 v153, v153, v157, s[24:25]\n"
    "buffer_store_dwordx2 v[152:153], v127, s[12:15], s69 offen sc1\n"
    "s_lshl1_add_u32 s43, s49, s52\n"
    "s_mul_i32 s66, s41, 0x3c00\n"
    "s_add_u32 m0, s66, 0x1000\n"
    "s_add_u32 s66, m0, s65\n"
    "buffer_load_dwordx4 v21, s[12:15], s66 offen sc1 lds\n"
    "s_lshl2_add_u32 s43, s43, s41\n"
    "s_mul_i32 s45, s43, 0x5000\n"
    "s_add_u32 s81, s45, 0x61c000\n"
    "s_add_u32 s82, s81, 0x4000\n"
    "v_lshlrev_b32 v1, 8, v9\n"
    "v_lshl_add_u32 v11, v8, 3, v1\n"
    "v_add_u32 v24, 0xf000, v7\n"
    "v_add_u32 v25, 0x1b800, v7\n"
    "v_lshlrev_b32 v32, 16, v212\n"
    "buffer_load_dwordx4 v7, s[12:15], s66 offen offset:1024 sc1 lds\n"
    "v_and_b32 v33, s58, v212\n"
    "v_lshlrev_b32 v34, 16, v213\n"
    "v_and_b32 v35, s58, v213\n"
    "v_lshlrev_b32 v36, 16, v214\n"
    "v_and_b32 v37, s58, v214\n"
    "v_lshlrev_b32 v38, 16, v215\n"
    "v_and_b32 v39, s58, v215\n"
    "v_lshlrev_b32 v40, 16, v216\n"
    "v_and_b32 v41, s58, v216\n"
    "v_lshlrev_b32 v42, 16, v217\n"
    "buffer_load_dwordx4 v7, s[12:15], s66 offen offset:2048 sc1 lds\n"
    "v_and_b32 v43, s58, v217\n"
    "v_lshlrev_b32 v44, 16, v218\n"
    "v_and_b32 v45, s58, v218\n"
    "v_lshlrev_b32 v46, 16, v219\n"
    "v_and_b32 v47, s58, v219\n"
    "v_lshlrev_b32 v48, 16, v220\n"
    "v_and_b32 v49, s58, v220\n"
    "v_lshlrev_b32 v50, 16, v221\n"
    "v_and_b32_e64 v51, s58, v221\n"
    "s_lshr_b32 s44, s51, 1\n"
    "buffer_load_dwordx4 v7, s[12:15], s66 offen offset:3072 sc1 lds\n"
    "s_mul_i32 s44, s44, 0x1400\n"
    "s_add_u32 s44, s44, s45\n"
    "s_add_u32 s44, s44, 0x89c000\n"
    "s_mul_i32 s45, s70, 0xc00\n"
    "s_add_u32 s60, s44, s45\n"
    "s_lshl_b32 s45, s70, 9\n"
    "s_add_u32 s76, s44, s45\n"
    "s_lshl3_add_u32 s75, s43, s51\n"
    "s_lshl_b32 s75, s75, 7\n"
    "s_add_u32 s75, s75, 0xb1c000\n"
    "s_mul_i32 s66, s41, 0x3c00\n"
    "s_add_u32 m0, s66, 0x2000\n"
    "s_add_u32 s66, m0, s65\n"
    "buffer_load_dwordx4 v7, s[12:15], s66 offen sc1 lds\n"
    "v_lshrrev_b32 v1, 1, v9\n"
    "v_lshlrev_b32 v1, 8, v1\n"
    "v_lshl_add_u32 v1, v8, 4, v1\n"
    "v_lshl_or_b32 v20, v9, 31, v1\n"
    "v_lshlrev_b32 v2, 3, v8\n"
    "v_lshl_or_b32 v23, v9, 30, v2\n"
    "v_lshlrev_b32 v1, 11, v0\n"
    "v_mov_b32 v2, 0x80000000\n"
    "v_cmp_gt_u32 s[28:29], 8, v0\n"
    "s_lshl4_add_u32 s63, s49, s52\n"
    "buffer_load_dwordx4 v21, s[12:15], s66 offen offset:1024 sc1 lds\n"
    "s_lshl_b32 s63, s63, 10\n"
    "s_add_u32 s63, s63, 0xc0000\n"
    "v_cndmask_b32 v17, v2, v1, s[28:29]\n"
    "s_waitcnt vmcnt(6)\n"
    "s_barrier\n"
    "s_cmp_lt_u32 s41, 1\n"
    "s_cbranch_scc0 .Lskip_17_%=\n"
    "buffer_store_dwordx2 v[18:19], v29, s[12:15], s56 offen sc1\n"
    ".Lskip_17_%=:\n"
    "buffer_load_dwordx4 v7, s[12:15], s66 offen offset:2048 sc1 lds\n"
    "s_nop 7\n"
    "buffer_load_dwordx4 v7, s[12:15], s66 offen offset:3072 sc1 lds\n"
    "s_nop 7\n"
    "s_mul_i32 s66, s41, 0x3c00\n"
    "s_add_u32 m0, s66, 0x3000\n"
    "s_add_u32 s66, m0, s65\n"
    "buffer_load_dwordx4 v7, s[12:15], s66 offen sc1 lds\n"
    "s_nop 7\n"
    "buffer_load_dwordx4 v7, s[12:15], s66 offen offset:1024 sc1 lds\n"
    "s_nop 7\n"
    "buffer_load_dwordx4 v21, s[12:15], s66 offen offset:2048 sc1 lds\n"
    "s_nop 7\n"
    "buffer_load_dwordx4 a[204:207], v7, s[12:15], s78 offen offset:3072 sc1\n"
    "s_add_u32 s78, s78, 0x1000\n"
    "s_nop 7\n"
    "buffer_load_dwordx4 a[200:203], v7, s[12:15], s78 offen sc1\n"
    "s_nop 7\n"
    "buffer_load_dwordx4 a[196:199], v7, s[12:15], s78 offen offset:1024 sc1\n"
    "s_nop 7\n"
    "buffer_load_dwordx4 a[192:195], v7, s[12:15], s78 offen offset:2048 sc1\n"
    "s_nop 7\n"
    "buffer_load_dwordx4 a[188:191], v7, s[12:15], s78 offen offset:3072 sc1\n"
    "s_add_u32 s78, s78, 0x1000\n"
    "s_sleep 1\n"
    "s_cmp_lt_u32 s41, 1\n"
    "s_cbranch_scc0 .Lskip_18_%=\n"
    "buffer_load_dwordx2 v[12:13], v17, s[12:15], s63 offen sc1\n"
    ".Lskip_18_%=:\n"
    "s_nop 7\n"
    "buffer_load_dwordx4 a[184:187], v7, s[12:15], s78 offen sc1\n"
    "s_nop 7\n"
    "buffer_load_dwordx4 a[180:183], v7, s[12:15], s78 offen offset:1024 sc1\n"
    "s_nop 7\n"
    "buffer_load_dwordx4 a[176:179], v7, s[12:15], s78 offen offset:2048 sc1\n"
    "s_nop 7\n"
    "buffer_load_dwordx4 a[172:175], v7, s[12:15], s78 offen offset:3072 sc1\n"
    "s_add_u32 s78, s78, 0x1000\n"
    "s_waitcnt vmcnt(4)\n"
    "v_cmp_ne_u64 vcc, s[38:39], v[12:13]\n"
    "s_nop 0\n"
    "s_and_b64 vcc, vcc, s[28:29]\n"
    "s_cbranch_scc0 .Lok_20_%=\n"
    "s_movk_i32 s46, 0x4e20\n"
    ".Lspin_19_%=:\n"
    "buffer_load_dwordx2 v[12:13], v17, s[12:15], s63 offen offset:0 sc1\n"
    "s_sub_u32 s46, s46, 1\n"
    "s_cbranch_scc1 .Lok_20_%=\n"
    "s_waitcnt vmcnt(0)\n"
    "v_cmp_ne_u64 vcc, s[38:39], v[12:13]\n"
    "s_nop 0\n"
    "s_and_b64 vcc, vcc, s[28:29]\n"
    "s_cbranch_scc1 .Lspin_19_%=\n"
    ".Lok_20_%=:\n"
    "s_barrier\n"
    "buffer_load_dwordx4 a[0:3], v7, s[12:15], s81 offen sc1\n"
    "ds_read_b128 a[92:95], v24\n"
    "buffer_load_dwordx4 a[4:7], v7, s[12:15], s81 offen offset:1024 sc1\n"
    "ds_read_b128 a[96:99], v24 offset:1024\n"
    "ds_read_b128 a[100:103], v24 offset:2048\n"
    "buffer_load_dwordx4 a[8:11], v7, s[12:15], s81 offen offset:2048 sc1\n"
    "ds_read_b128 a[104:107], v24 offset:3072\n"
    "ds_read_b128 a[108:111], v24 offset:4096\n"
    "buffer_load_dwordx4 a[12:15], v7, s[12:15], s81 offen offset:3072 sc1\n"
    "s_add_u32 s81, s81, 0x1000\n"
    "ds_read_b128 a[112:115], v24 offset:5120\n"
    "ds_read_b128 a[116:119], v24 offset:6144\n"
    "buffer_load_dwordx2 a[28:29], v11, s[12:15], s82 offen sc1\n"
    "buffer_load_dwordx2 a[30:31], v11, s[12:15], s82 offen offset:128 sc1\n"
    "ds_read_b128 a[120:123], v24 offset:7168\n"
    "ds_read_b128 a[124:127], v24 offset:8192\n"
    "buffer_load_dwordx4 a[32:35], v7, s[12:15], s81 offen sc1\n"
    "ds_read_b128 a[128:131], v24 offset:9216\n"
    "ds_read_b128 a[132:135], v24 offset:10240\n"
    "buffer_load_dwordx4 a[36:39], v7, s[12:15], s81 offen offset:1024 sc1\n"
    "ds_read_b128 a[136:139], v24 offset:11264\n"
    "ds_read_b128 a[140:143], v24 offset:12288\n"
    "buffer_load_dwordx4 a[40:43], v7, s[12:15], s81 offen offset:2048 sc1\n"
    "ds_read_b128 a[144:147], v24 offset:13312\n"
    "ds_read_b128 a[148:151], v24 offset:14336\n"
    "s_waitcnt vmcnt(8) lgkmcnt(14)\n"
    "v_mfma_f32_16x16x32_bf16 v[32:35], a[92:95], a[0:3], v[32:35]\n"
    "ds_read_b128 a[152:155], v24 offset:15360\n"
    "s_waitcnt lgkmcnt(14)\n"
    "v_mfma_f32_16x16x32_bf16 v[36:39], a[96:99], a[0:3], v[36:39]\n"
    "ds_read_b128 a[156:159], v24 offset:16384\n"
    "buffer_load_dwordx4 a[44:47], v7, s[12:15], s81 offen offset:3072 sc1\n"
    "s_add_u32 s81, s81, 0x1000\n"
    "s_waitcnt lgkmcnt(14)\n"
    "v_mfma_f32_16x16x32_bf16 v[40:43], a[100:103], a[0:3], v[40:43]\n"
    "ds_read_b128 a[160:163], v24 offset:17408\n"
    "s_waitcnt lgkmcnt(14)\n"
    "v_mfma_f32_16x16x32_bf16 v[44:47], a[104:107], a[0:3], v[44:47]\n"
    "ds_read_b128 a[164:167], v24 offset:18432\n"
    "s_waitcnt lgkmcnt(14)\n"
    "v_mfma_f32_16x16x32_bf16 v[48:51], a[108:111], a[0:3], v[48:51]\n"
    "ds_read_b128 a[168:171], v24 offset:19456\n"
    "s_waitcnt vmcnt(8) lgkmcnt(10)\n"
    "v_mfma_f32_16x16x32_bf16 v[32:35], a[112:115], a[4:7], v[32:35]\n"
    "ds_read_b128 a[92:95], v24 offset:20480\n"
    "v_mfma_f32_16x16x32_bf16 v[36:39], a[116:119], a[4:7], v[36:39]\n"
    "ds_read_b128 a[96:99], v24 offset:21504\n"
    "buffer_load_dwordx2 a[48:49], v11, s[12:15], s82 offen offset:1024 sc1\n"
    "buffer_load_dwordx2 a[50:51], v11, s[12:15], s82 offen offset:1152 sc1\n"
    "v_mfma_f32_16x16x32_bf16 v[40:43], a[120:123], a[4:7], v[40:43]\n"
    "ds_read_b128 a[100:103], v24 offset:22528\n"
    "v_mfma_f32_16x16x32_bf16 v[44:47], a[124:127], a[4:7], v[44:47]\n"
    "ds_read_b128 a[104:107], v24 offset:23552\n"
    "v_mfma_f32_16x16x32_bf16 v[48:51], a[128:131], a[4:7], v[48:51]\n"
    "ds_read_b128 a[108:111], v24 offset:24576\n"
    "s_waitcnt vmcnt(9) lgkmcnt(10)\n"
    "v_mfma_f32_16x16x32_bf16 v[32:35], a[132:135], a[8:11], v[32:35]\n"
    "ds_read_b128 a[112:115], v24 offset:25600\n"
    "v_mfma_f32_16x16x32_bf16 v[36:39], a[136:139], a[8:11], v[36:39]\n"
    "ds_read_b128 a[116:119], v24 offset:26624\n"
    "buffer_load_dwordx4 a[52:55], v7, s[12:15], s81 offen sc1\n"
    "v_mfma_f32_16x16x32_bf16 v[40:43], a[140:143], a[8:11], v[40:43]\n"
    "ds_read_b128 a[120:123], v24 offset:27648\n"
    "v_mfma_f32_16x16x32_bf16 v[44:47], a[144:147], a[8:11], v[44:47]\n"
    "ds_read_b128 a[124:127], v24 offset:28672\n"
    "v_mfma_f32_16x16x32_bf16 v[48:51], a[148:151], a[8:11], v[48:51]\n"
    "ds_read_b128 a[128:131], v24 offset:29696\n"
    "s_waitcnt vmcnt(9) lgkmcnt(10)\n"
    "v_mfma_f32_16x16x32_bf16 v[32:35], a[152:155], a[12:15], v[32:35]\n"
    "ds_read_b128 a[132:135], v24 offset:30720\n"
    "v_mfma_f32_16x16x32_bf16 v[36:39], a[156:159], a[12:15], v[36:39]\n"
    "ds_read_b128 a[136:139], v24 offset:31744\n"
    "buffer_load_dwordx4 a[56:59], v7, s[12:15], s81 offen offset:1024 sc1\n"
    "v_mfma_f32_16x16x32_bf16 v[40:43], a[160:163], a[12:15], v[40:43]\n"
    "ds_read_b128 a[140:143], v24 offset:32768\n"
    "v_mfma_f32_16x16x32_bf16 v[44:47], a[164:167], a[12:15], v[44:47]\n"
    "ds_read_b128 a[144:147], v24 offset:33792\n"
    "v_mfma_f32_16x16x32_bf16 v[48:51], a[168:171], a[12:15], v[48:51]\n"
    "ds_read_b128 a[148:151], v24 offset:34816\n"
    "s_waitcnt vmcnt(8) lgkmcnt(10)\n"
    "v_mfma_f32_16x16x32_bf16 v[32:35], a[92:95], a[28:31], v[32:35]\n"
    "ds_read_b128 a[152:155], v24 offset:35840\n"
    "v_mfma_f32_16x16x32_bf16 v[36:39], a[96:99], a[28:31], v[36:39]\n"
    "ds_read_b128 a[156:159], v24 offset:36864\n"
    "buffer_load_dwordx4 a[60:63], v7, s[12:15], s81 offen offset:2048 sc1\n"
    "v_mfma_f32_16x16x32_bf16 v[40:43], a[100:103], a[28:31], v[40:43]\n"
    "ds_read_b128 a[160:163], v24 offset:37888\n"
    "v_mfma_f32_16x16x32_bf16 v[44:47], a[104:107], a[28:31], v[44:47]\n"
    "ds_read_b128 a[164:167], v24 offset:38912\n"
    "v_mfma_f32_16x16x32_bf16 v[48:51], a[108:111], a[28:31], v[48:51]\n"
    "ds_read_b128 a[168:171], v24 offset:39936\n"
    "s_waitcnt vmcnt(8) lgkmcnt(10)\n"
    "v_mfma_f32_16x16x32_bf16 v[32:35], a[112:115], a[32:35], v[32:35]\n"
    "ds_read_b128 a[92:95], v24 offset:40960\n"
    "v_mfma_f32_16x16x32_bf16 v[36:39], a[116:119], a[32:35], v[36:39]\n"
    "ds_read_b128 a[96:99], v24 offset:41984\n"
    "buffer_load_dwordx4 a[64:67], v7, s[12:15], s81 offen offset:3072 sc1\n"
    "s_add_u32 s81, s81, 0x1000\n"
    "v_mfma_f32_16x16x32_bf16 v[40:43], a[120:123], a[32:35], v[40:43]\n"
    "ds_read_b128 a[100:103], v24 offset:43008\n"
    "v_mfma_f32_16x16x32_bf16 v[44:47], a[124:127], a[32:35], v[44:47]\n"
    "ds_read_b128 a[104:107], v24 offset:44032\n"
    "v_mfma_f32_16x16x32_bf16 v[48:51], a[128:131], a[32:35], v[48:51]\n"
    "ds_read_b128 a[108:111], v24 offset:45056\n"
    "s_waitcnt vmcnt(8) lgkmcnt(10)\n"
    "v_mfma_f32_16x16x32_bf16 v[32:35], a[132:135], a[36:39], v[32:35]\n"
    "ds_read_b128 a[112:115], v24 offset:46080\n"
    "v_mfma_f32_16x16x32_bf16 v[36:39], a[136:139], a[36:39], v[36:39]\n"
    "ds_read_b128 a[116:119], v24 offset:47104\n"
    "buffer_load_dwordx2 a[68:69], v11, s[12:15], s82 offen offset:2048 sc1\n"
    "buffer_load_dwordx2 a[70:71], v11, s[12:15], s82 offen offset:2176 sc1\n"
    "v_mfma_f32_16x16x32_bf16 v[40:43], a[140:143], a[36:39], v[40:43]\n"
    "ds_read_b128 a[120:123], v24 offset:48128\n"
    "v_mfma_f32_16x16x32_bf16 v[44:47], a[144:147], a[36:39], v[44:47]\n"
    "ds_read_b128 a[124:127], v24 offset:49152\n"
    "v_mfma_f32_16x16x32_bf16 v[48:51], a[148:151], a[36:39], v[48:51]\n"
    "ds_read_b128 a[128:131], v24 offset:50176\n"
    "s_waitcnt vmcnt(9) lgkmcnt(10)\n"
    "v_mfma_f32_16x16x32_bf16 v[32:35], a[152:155], a[40:43], v[32:35]\n"
    "ds_read_b128 a[132:135], v25\n"
    "v_mfma_f32_16x16x32_bf16 v[36:39], a[156:159], a[40:43], v[36:39]\n"
    "ds_read_b128 a[136:139], v25 offset:1024\n"
    "buffer_load_dwordx4 a[72:75], v7, s[12:15], s81 offen sc1\n"
    "v_mfma_f32_16x16x32_bf16 v[40:43], a[160:163], a[40:43], v[40:43]\n"
    "ds_read_b128 a[140:143], v25 offset:2048\n"
    "v_mfma_f32_16x16x32_bf16 v[44:47], a[164:167], a[40:43], v[44:47]\n"
    "ds_read_b128 a[144:147], v25 offset:3072\n"
    "v_mfma_f32_16x16x32_bf16 v[48:51], a[168:171], a[40:43], v[48:51]\n"
    "ds_read_b128 a[148:151], v25 offset:4096\n"
    "s_waitcnt vmcnt(9) lgkmcnt(10)\n"
    "v_mfma_f32_16x16x32_bf16 v[32:35], a[92:95], a[44:47], v[32:35]\n"
    "ds_read_b128 a[152:155], v25 offset:5120\n"
    "v_mfma_f32_16x16x32_bf16 v[36:39], a[96:99], a[44:47], v[36:39]\n"
    "ds_read_b128 a[156:159], v25 offset:6144\n"
    "buffer_load_dwordx4 a[76:79], v7, s[12:15], s81 offen offset:1024 sc1\n"
    "v_mfma_f32_16x16x32_bf16 v[40:43], a[100:103], a[44:47], v[40:43]\n"
    "ds_read_b128 a[160:163], v25 offset:7168\n"
    "v_mfma_f32_16x16x32_bf16 v[44:47], a[104:107], a[44:47], v[44:47]\n"
    "ds_read_b128 a[164:167], v25 offset:8192\n"
    "v_mfma_f32_16x16x32_bf16 v[48:51], a[108:111], a[44:47], v[48:51]\n"
    "ds_read_b128 a[168:171], v25 offset:9216\n"
    "s_waitcnt vmcnt(8) lgkmcnt(10)\n"
    "v_mfma_f32_16x16x32_bf16 v[32:35], a[112:115], a[48:51], v[32:35]\n"
    "ds_read_b128 a[92:95], v25 offset:10240\n"
    "v_mfma_f32_16x16x32_bf16 v[36:39], a[116:119], a[48:51], v[36:39]\n"
    "ds_read_b128 a[96:99], v25 offset:11264\n"
    "buffer_load_dwordx4 a[80:83], v7, s[12:15], s81 offen offset:2048 sc1\n"
    "v_mfma_f32_16x16x32_bf16 v[40:43], a[120:123], a[48:51], v[40:43]\n"
    "ds_read_b128 a[100:103], v25 offset:12288\n"
    "v_mfma_f32_16x16x32_bf16 v[44:47], a[124:127], a[48:51], v[44:47]\n"
    "ds_read_b128 a[104:107], v25 offset:13312\n"
    "v_mfma_f32_16x16x32_bf16 v[48:51], a[128:131], a[48:51], v[48:51]\n"
    "ds_read_b128 a[108:111], v25 offset:14336\n"
    "s_waitcnt vmcnt(8) lgkmcnt(10)\n"
    "v_mfma_f32_16x16x32_bf16 v[32:35], a[132:135], a[52:55], v[32:35]\n"
    "ds_read_b128 a[112:115], v25 offset:15360\n"
    "v_mfma_f32_16x16x32_bf16 v[36:39], a[136:139], a[52:55], v[36:39]\n"
    "ds_read_b128 a[116:119], v25 offset:16384\n"
    "buffer_load_dwordx4 a[84:87], v7, s[12:15], s81 offen offset:3072 sc1\n"
    "s_add_u32 s81, s81, 0x1000\n"
    "v_mfma_f32_16x16x32_bf16 v[40:43], a[140:143], a[52:55], v[40:43]\n"
    "ds_read_b128 a[120:123], v25 offset:17408\n"
    "v_mfma_f32_16x16x32_bf16 v[44:47], a[144:147], a[52:55], v[44:47]\n"
    "ds_read_b128 a[124:127], v25 offset:18432\n"
    "v_mfma_f32_16x16x32_bf16 v[48:51], a[148:151], a[52:55], v[48:51]\n"
    "ds_read_b128 a[128:131], v25 offset:19456\n"
    "s_waitcnt vmcnt(8) lgkmcnt(10)\n"
    "v_mfma_f32_16x16x32_bf16 v[32:35], a[152:155], a[56:59], v[32:35]\n"
    "ds_read_b128 a[132:135], v25 offset:20480\n"
    "v_mfma_f32_16x16x32_bf16 v[36:39], a[156:159], a[56:59], v[36:39]\n"
    "ds_read_b128 a[136:139], v25 offset:21504\n"
    "buffer_load_dwordx2 a[88:89], v11, s[12:15], s82 offen offset:3072 sc1\n"
    "buffer_load_dwordx2 a[90:91], v11, s[12:15], s82 offen offset:3200 sc1\n"
    "v_mfma_f32_16x16x32_bf16 v[40:43], a[160:163], a[56:59], v[40:43]\n"
    "ds_read_b128 a[140:143], v25 offset:22528\n"
    "v_mfma_f32_16x16x32_bf16 v[44:47], a[164:167], a[56:59], v[44:47]\n"
    "ds_read_b128 a[144:147], v25 offset:23552\n"
    "v_mfma_f32_16x16x32_bf16 v[48:51], a[168:171], a[56:59], v[48:51]\n"
    "ds_read_b128 a[148:151], v25 offset:24576\n"
    "s_waitcnt vmcnt(9) lgkmcnt(10)\n"
    "v_mfma_f32_16x16x32_bf16 v[32:35], a[92:95], a[60:63], v[32:35]\n"
    "ds_read_b128 a[152:155], v25 offset:25600\n"
    "v_mfma_f32_16x16x32_bf16 v[36:39], a[96:99], a[60:63], v[36:39]\n"
    "ds_read_b128 a[156:159], v25 offset:26624\n"
    "buffer_load_dwordx4 v[252:255], v7, s[12:15], s78 offen sc1\n"
    "v_mfma_f32_16x16x32_bf16 v[40:43], a[100:103], a[60:63], v[40:43]\n"
    "ds_read_b128 a[160:163], v25 offset:27648\n"
    "v_mfma_f32_16x16x32_bf16 v[44:47], a[104:107], a[60:63], v[44:47]\n"
    "ds_read_b128 a[164:167], v25 offset:28672\n"
    "v_mfma_f32_16x16x32_bf16 v[48:51], a[108:111], a[60:63], v[48:51]\n"
    "ds_read_b128 a[168:171], v25 offset:29696\n"
    "s_waitcnt vmcnt(9) lgkmcnt(10)\n"
    "v_mfma_f32_16x16x32_bf16 v[32:35], a[112:115], a[64:67], v[32:35]\n"
    "ds_read_b128 a[92:95], v25 offset:30720\n"
    "v_mfma_f32_16x16x32_bf16 v[36:39], a[116:119], a[64:67], v[36:39]\n"
    "ds_read_b128 a[96:99], v25 offset:31744\n"
    "buffer_load_dwordx4 v[248:251], v7, s[12:15], s78 offen offset:1024 sc1\n"
    "v_mfma_f32_16x16x32_bf16 v[40:43], a[120:123], a[64:67], v[40:43]\n"
    "ds_read_b128 a[100:103], v25 offset:32768\n"
    "v_mfma_f32_16x16x32_bf16 v[44:47], a[124:127], a[64:67], v[44:47]\n"
    "ds_read_b128 a[104:107], v25 offset:33792\n"
    "v_mfma_f32_16x16x32_bf16 v[48:51], a[128:131], a[64:67], v[48:51]\n"
    "ds_read_b128 a[108:111], v25 offset:34816\n"
    "s_waitcnt vmcnt(8) lgkmcnt(10)\n"
    "v_mfma_f32_16x16x32_bf16 v[32:35], a[132:135], a[68:71], v[32:35]\n"
    "ds_read_b128 a[112:115], v25 offset:35840\n"
    "v_mfma_f32_16x16x32_bf16 v[36:39], a[136:139], a[68:71], v[36:39]\n"
    "ds_read_b128 a[116:119], v25 offset:36864\n"
    "buffer_load_dwordx4 v[244:247], v7, s[12:15], s78 offen offset:2048 sc1\n"
    "v_mfma_f32_16x16x32_bf16 v[40:43], a[140:143], a[68:71], v[40:43]\n"
    "ds_read_b128 a[120:123], v25 offset:37888\n"
    "v_mfma_f32_16x16x32_bf16 v[44:47], a[144:147], a[68:71], v[44:47]\n"
    "ds_read_b128 a[124:127], v25 offset:38912\n"
    "v_mfma_f32_16x16x32_bf16 v[48:51], a[148:151], a[68:71], v[48:51]\n"
    "ds_read_b128 a[128:131], v25 offset:39936\n"
    "s_waitcnt vmcnt(8) lgkmcnt(10)\n"
    "v_mfma_f32_16x16x32_bf16 v[32:35], a[152:155], a[72:75], v[32:35]\n"
    "ds_read_b128 a[132:135], v25 offset:40960\n"
    "v_mfma_f32_16x16x32_bf16 v[36:39], a[156:159], a[72:75], v[36:39]\n"
    "ds_read_b128 a[136:139], v25 offset:41984\n"
    "buffer_load_dwordx4 v[240:243], v7, s[12:15], s78 offen offset:3072 sc1\n"
    "s_add_u32 s78, s78, 0x1000\n"
    "v_mfma_f32_16x16x32_bf16 v[40:43], a[160:163], a[72:75], v[40:43]\n"
    "ds_read_b128 a[140:143], v25 offset:43008\n"
    "v_mfma_f32_16x16x32_bf16 v[44:47], a[164:167], a[72:75], v[44:47]\n"
    "ds_read_b128 a[144:147], v25 offset:44032\n"
    "v_mfma_f32_16x16x32_bf16 v[48:51], a[168:171], a[72:75], v[48:51]\n"
    "ds_read_b128 a[148:151], v25 offset:45056\n"
    "s_waitcnt vmcnt(8) lgkmcnt(10)\n"
    "v_mfma_f32_16x16x32_bf16 v[32:35], a[92:95], a[76:79], v[32:35]\n"
    "ds_read_b128 a[152:155], v25 offset:46080\n"
    "v_mfma_f32_16x16x32_bf16 v[36:39], a[96:99], a[76:79], v[36:39]\n"
    "ds_read_b128 a[156:159], v25 offset:47104\n"
    "buffer_load_dwordx4 v[236:239], v7, s[12:15], s78 offen sc1\n"
    "v_mfma_f32_16x16x32_bf16 v[40:43], a[100:103], a[76:79], v[40:43]\n"
    "ds_read_b128 a[160:163], v25 offset:48128\n"
    "v_mfma_f32_16x16x32_bf16 v[44:47], a[104:107], a[76:79], v[44:47]\n"
    "ds_read_b128 a[164:167], v25 offset:49152\n"
    "v_mfma_f32_16x16x32_bf16 v[48:51], a[108:111], a[76:79], v[48:51]\n"
    "ds_read_b128 a[168:171], v25 offset:50176\n"
    "s_waitcnt vmcnt(8) lgkmcnt(10)\n"
    "v_mfma_f32_16x16x32_bf16 v[32:35], a[112:115], a[80:83], v[32:35]\n"
    "v_mfma_f32_16x16x32_bf16 v[36:39], a[116:119], a[80:83], v[36:39]\n"
    "buffer_load_dwordx4 v[232:235], v7, s[12:15], s78 offen offset:1024 sc1\n"
    "v_mfma_f32_16x16x32_bf16 v[40:43], a[120:123], a[80:83], v[40:43]\n"
    "v_mfma_f32_16x16x32_bf16 v[44:47], a[124:127], a[80:83], v[44:47]\n"
    "v_mfma_f32_16x16x32_bf16 v[48:51], a[128:131], a[80:83], v[48:51]\n"
    "s_waitcnt vmcnt(8) lgkmcnt(5)\n"
    "v_mfma_f32_16x16x32_bf16 v[32:35], a[132:135], a[84:87], v[32:35]\n"
    "v_mfma_f32_16x16x32_bf16 v[36:39], a[136:139], a[84:87], v[36:39]\n"
    "buffer_load_dwordx4 v[228:231], v7, s[12:15], s78 offen offset:2048 sc1\n"
    "v_mfma_f32_16x16x32_bf16 v[40:43], a[140:143], a[84:87], v[40:43]\n"
    "v_mfma_f32_16x16x32_bf16 v[44:47], a[144:147], a[84:87], v[44:47]\n"
    "v_mfma_f32_16x16x32_bf16 v[48:51], a[148:151], a[84:87], v[48:51]\n"
    "s_waitcnt vmcnt(7) lgkmcnt(0)\n"
    "v_mfma_f32_16x16x32_bf16 v[32:35], a[152:155], a[88:91], v[32:35]\n"
    "v_mfma_f32_16x16x32_bf16 v[36:39], a[156:159], a[88:91], v[36:39]\n"
    "buffer_load_dwordx4 v[224:227], v7, s[12:15], s78 offen offset:3072 sc1\n"
    "s_add_u32 s78, s78, 0x1000\n"
    "v_mfma_f32_16x16x32_bf16 v[40:43], a[160:163], a[88:91], v[40:43]\n"
    "v_mfma_f32_16x16x32_bf16 v[44:47], a[164:167], a[88:91], v[44:47]\n"
    "v_mfma_f32_16x16x32_bf16 v[48:51], a[168:171], a[88:91], v[48:51]\n"
    "s_nop 1\n"
    "v_cvt_pk_bf16_f32 v212, v32, v33\n"
    "v_cvt_pk_bf16_f32 v213, v34, v35\n"
    "v_cvt_pk_bf16_f32 v214, v36, v37\n"
    "v_cvt_pk_bf16_f32 v215, v38, v39\n"
    "buffer_store_dwordx4 v[212:215], v7, s[12:15], s60 offen sc1\n"
    "v_cvt_pk_bf16_f32 v216, v40, v41\n"
    "v_cvt_pk_bf16_f32 v217, v42, v43\n"
    "v_cvt_pk_bf16_f32 v218, v44, v45\n"
    "v_cvt_pk_bf16_f32 v219, v46, v47\n"
    "v_cvt_pk_bf16_f32 v220, v48, v49\n"
    "v_cvt_pk_bf16_f32 v221, v50, v51\n"
    "buffer_store_dwordx4 v[216:219], v7, s[12:15], s60 offen offset:1024 sc1\n"
    "v_mov_b32 v32, v220\n"
    "v_mov_b32 v33, v221\n"
    "v_mov_b32 v34, v220\n"
    "v_mov_b32_e64 v35, v221\n"
    "v_dot2_f32_bf16 v36, v212, s57, 0\n"
    "v_dot2_f32_bf16 v37, v212, v212, 0\n"
    "v_permlane16_swap_b32 v32, v34\n"
    "v_permlane16_swap_b32 v33, v35\n"
    "buffer_store_dwordx4 v[32:35], v20, s[12:15], s76 offen offset:2048 sc1\n"
    "v_dot2_f32_bf16 v36, v213, s57, v36\n"
    "v_dot2_f32_bf16 v37, v213, v213, v37\n"
    "v_dot2_f32_bf16 v36, v214, s57, v36\n"
    "v_dot2_f32_bf16 v37, v214, v214, v37\n"
    "v_dot2_f32_bf16 v36, v215, s57, v36\n"
    "v_dot2_f32_bf16 v37, v215, v215, v37\n"
    "v_dot2_f32_bf16 v36, v216, s57, v36\n"
    "v_dot2_f32_bf16 v37, v216, v216, v37\n"
    "v_dot2_f32_bf16 v36, v217, s57, v36\n"
    "v_dot2_f32_bf16 v37, v217, v217, v37\n"
    "v_dot2_f32_bf16 v36, v218, s57, v36\n"
    "v_dot2_f32_bf16 v37, v218, v218, v37\n"
    "v_dot2_f32_bf16 v36, v219, s57, v36\n"
    "v_dot2_f32_bf16 v37, v219, v219, v37\n"
    "v_dot2_f32_bf16 v36, v220, s57, v36\n"
    "v_dot2_f32_bf16 v37, v220, v220, v37\n"
    "v_dot2_f32_bf16 v36, v221, s57, v36\n"
    "v_dot2_f32_bf16 v37, v221, v221, v37\n"
    "s_nop 1\n"
    "v_mov_b32 v38, v36\n"
    "v_mov_b32 v39, v37\n"
    "s_nop 0\n"
    "v_permlane32_swap_b32 v36, v38\n"
    "v_permlane32_swap_b32 v37, v39\n"
    "v_pk_add_f32 v[36:37], v[36:37], v[38:39]\n"
    "v_mov_b32 v38, v36\n"
    "v_mov_b32 v39, v37\n"
    "s_nop 0\n"
    "v_permlane16_swap_b32 v36, v38\n"
    "v_permlane16_swap_b32 v37, v39\n"
    "v_pk_add_f32 v[36:37], v[36:37], v[38:39]\n"
    "buffer_store_dwordx2 v[36:37], v23, s[12:15], s75 offen sc1\n"
    "s_lshl2_add_u32 s43, s49, s41\n"
    "buffer_load_dwordx4 v[208:211], v7, s[12:15], s78 offen sc1\n"
    "s_mul_i32 s67, s43, 0xa000\n"
    "s_add_u32 s67, s67, 0x89c000\n"
    "s_add_u32 s68, s67, 0x5000\n"
    "s_lshl_b32 s69, s43, 11\n"
    "s_add_u32 s69, s69, 0xb1c000\n"
    "s_mul_i32 s43, s43, 0x1b000\n"
    "s_add_u32 s43, s43, 0xb3c000\n"
    "s_lshr_b32 s44, s50, 2\n"
    "s_and_b32 s45, s50, 3\n"
    "buffer_load_dwordx4 v[204:207], v7, s[12:15], s78 offen offset:1024 sc1\n"
    "s_mul_i32 s46, s44, 13\n"
    "s_lshl1_add_u32 s88, s45, s46\n"
    "s_lshl_b32 s88, s88, 10\n"
    "s_add_u32 s88, s88, s43\n"
    "s_add_u32 s83, s88, 0xd800\n"
    "s_add_u32 s81, s46, s45\n"
    "s_add_u32 s81, s81, 9\n"
    "s_lshl_b32 s81, s81, 10\n"
    "s_add_u32 s81, s81, s43\n"
    "s_add_u32 s46, s46, 8\n"
    "buffer_load_dwordx4 v[200:203], v7, s[12:15], s78 offen offset:2048 sc1\n"
    "s_lshl1_add_u32 s46, s46, s45\n"
    "s_add_u32 s44, s44, 0x68\n"
    "s_cmp_eq_u32 s45, 2\n"
    "s_cselect_b32 s46, s44, s46\n"
    "s_lshl_b32 s46, s46, 9\n"
    "s_add_u32 s82, s46, s43\n"
    "s_cmp_eq_u32 s45, 3\n"
    "s_cselect_b32 s82, 0x1454000, s82\n"
    "v_add_u32 v26, 0xc800, v7\n"
    "v_lshlrev_b32 v2, 8, v9\n"
    "v_lshl_add_u32 v23, v8, 3, v2\n"
    "v_lshlrev_b32_e64 v1, 10, v0\n"
    "v_mov_b32 v2, 0x80000000\n"
    "v_cmp_gt_u32 s[28:29], 16, v0\n"
    "s_lshl_b32 s63, s49, 14\n"
    "s_add_u32 s63, s63, 0xc0000\n"
    "buffer_load_dwordx4 v[196:199], v7, s[12:15], s78 offen offset:3072 sc1\n"
    "s_add_u32 s78, s78, 0x1000\n"
    "v_cndmask_b32 v17, v2, v1, s[28:29]\n"
    "v_and_b32 v1, 1, v9\n"
    "v_mul_u32_u24 v1, 0xd800, v1\n"
    "v_lshrrev_b32 v2, 1, v9\n"
    "v_lshl_add_u32 v1, v2, 8, v1\n"
    "v_lshl_add_u32 v27, v8, 4, v1\n"
    "s_nop 1\n"
    "buffer_load_dwordx4 v[192:195], v7, s[12:15], s78 offen sc1\n"
    "s_nop 7\n"
    "buffer_load_dwordx4 v[188:191], v7, s[12:15], s78 offen offset:1024 sc1\n"
    "s_nop 7\n"
    "buffer_load_dwordx4 v[184:187], v7, s[12:15], s78 offen offset:2048 sc1\n"
    "s_nop 7\n"
    "buffer_load_dwordx4 v[180:183], v7, s[12:15], s78 offen offset:3072 sc1\n"
    "s_add_u32 s78, s78, 0x1000\n"
    "s_waitcnt vmcnt(8)\n"
    "s_barrier\n"
    "s_cmp_lt_u32 s41, 1\n"
    "s_cbranch_scc0 .Lskip_21_%=\n"
    "buffer_store_dwordx2 v[18:19], v29, s[12:15], s56 offen offset:128 sc1\n"
    ".Lskip_21_%=:\n"
    "s_mul_i32 s66, s41, 0x2800\n"
    "s_add_u32 m0, s66, 0xf000\n"
    "s_add_u32 s66, m0, s65\n"
    "buffer_load_dwordx4 v7, s[12:15], s66 offen sc1 lds\n"
    "s_nop 7\n"
    "buffer_load_dwordx4 v7, s[12:15], s66 offen offset:1024 sc1 lds\n"
    "s_nop 7\n"
    "buffer_load_dwordx4 v7, s[12:15], s66 offen offset:2048 sc1 lds\n"
    "s_nop 7\n"
    "buffer_load_dwordx4 v7, s[12:15], s66 offen offset:3072 sc1 lds\n"
    "s_nop 7\n"
    "s_mul_i32 s66, s41, 0x2800\n"
    "s_add_u32 m0, s66, 0x10000\n"
    "s_add_u32 s66, m0, s65\n"
    "buffer_load_dwordx4 v21, s[12:15], s66 offen sc1 lds\n"
    "s_nop 7\n"
    "buffer_load_dwordx4 v7, s[12:15], s66 offen offset:1024 sc1 lds\n"
    "s_nop 7\n"
    "buffer_load_dwordx4 v7, s[12:15], s66 offen offset:2048 sc1 lds\n"
    "s_nop 7\n"
    "buffer_load_dwordx4 v7, s[12:15], s66 offen offset:3072 sc1 lds\n"
    "s_nop 7\n"
    "s_mul_i32 s66, s41, 0x2800\n"
    "s_add_u32 m0, s66, 0x11000\n"
    "s_add_u32 s66, m0, s65\n"
    "buffer_load_dwordx4 v7, s[12:15], s66 offen sc1 lds\n"
    "s_nop 7\n"
    "buffer_load_dwordx4 v21, s[12:15], s66 offen offset:1024 sc1 lds\n"
    "s_sleep 1\n"
    "s_cmp_lt_u32 s41, 1\n"
    "s_cbranch_scc0 .Lskip_22_%=\n"
    "buffer_load_dwordx2 v[12:13], v17, s[12:15], s63 offen offset:128 sc1\n"
    ".Lskip_22_%=:\n"
    "s_nop 7\n"
    "s_cmp_lt_u32 s41, 2\n"
    "s_cbranch_scc0 .Lskip_23_%=\n"
    "s_mul_i32 s66, s41, 0x400\n"
    "s_add_u32 m0, s66, 0x23000\n"
    "s_add_u32 s66, m0, s80\n"
    "buffer_load_dwordx4 v22, s[12:15], s66 offen sc1 lds\n"
    ".Lskip_23_%=:\n"
    "s_nop 7\n"
    "s_mul_i32 s66, s41, 0x2800\n"
    "s_add_u32 m0, s66, 0x19000\n"
    "s_add_u32 s66, m0, s65\n"
    "buffer_load_dwordx4 v7, s[12:15], s66 offen sc1 lds\n"
    "s_nop 7\n"
    "buffer_load_dwordx4 v7, s[12:15], s66 offen offset:1024 sc1 lds\n"
    "s_nop 7\n"
    "buffer_load_dwordx4 v7, s[12:15], s66 offen offset:2048 sc1 lds\n"
    "s_nop 7\n"
    "buffer_load_dwordx4 v7, s[12:15], s66 offen offset:3072 sc1 lds\n"
    "s_waitcnt vmcnt(4)\n"
    "v_cmp_ne_u64 vcc, s[38:39], v[12:13]\n"
    "s_nop 0\n"
    "s_and_b64 vcc, vcc, s[28:29]\n"
    "s_cbranch_scc0 .Lok_25_%=\n"
    "s_movk_i32 s46, 0x4e20\n"
    ".Lspin_24_%=:\n"
    "buffer_load_dwordx2 v[12:13], v17, s[12:15], s63 offen offset:128 sc1\n"
    "s_sub_u32 s46, s46, 1\n"
    "s_cbranch_scc1 .Lok_25_%=\n"
    "s_waitcnt vmcnt(0)\n"
    "v_cmp_ne_u64 vcc, s[38:39], v[12:13]\n"
    "s_nop 0\n"
    "s_and_b64 vcc, vcc, s[28:29]\n"
    "s_cbranch_scc1 .Lspin_24_%=\n"
    ".Lok_25_%=:\n"
    "s_barrier\n"
    "buffer_load_dwordx4 a[0:3], v7, s[12:15], s67 offen sc1\n"
    "buffer_load_dwordx4 a[92:95], v7, s[12:15], s68 offen sc1\n"
    "buffer_load_dwordx4 a[4:7], v7, s[12:15], s67 offen offset:1024 sc1\n"
    "buffer_load_dwordx4 a[96:99], v7, s[12:15], s68 offen offset:1024 sc1\n"
    "ds_read_b128 v[32:35], v7\n"
    "ds_read_b128 v[36:39], v7 offset:1024\n"
    "ds_read_b128 v[40:43], v7 offset:2048\n"
    "ds_read_b128 v[44:47], v7 offset:3072\n"
    "ds_read_b128 v[48:51], v7 offset:4096\n"
    "ds_read_b128 v[52:55], v7 offset:5120\n"
    "buffer_load_dwordx4 a[8:11], v7, s[12:15], s67 offen offset:2048 sc1\n"
    "buffer_load_dwordx4 a[100:103], v7, s[12:15], s68 offen offset:2048 sc1\n"
    "buffer_load_dwordx4 a[12:15], v7, s[12:15], s67 offen offset:3072 sc1\n"
    "s_add_u32 s67, s67, 0x1000\n"
    "buffer_load_dwordx4 a[104:107], v7, s[12:15], s68 offen offset:3072 sc1\n"
    "s_add_u32 s68, s68, 0x1000\n"
    "buffer_load_dwordx4 a[28:31], v7, s[12:15], s67 offen sc1\n"
    "buffer_load_dwordx4 a[108:111], v7, s[12:15], s68 offen sc1\n"
    "buffer_load_dwordx4 a[32:35], v7, s[12:15], s67 offen offset:1024 sc1\n"
    "buffer_load_dwordx4 a[112:115], v7, s[12:15], s68 offen offset:1024 sc1\n"
    "s_waitcnt vmcnt(11) lgkmcnt(5)\n"
    "v_mfma_f32_16x16x32_bf16 v[100:103], v[32:35], a[0:3], 0\n"
    "ds_read_b128 v[56:59], v7 offset:6144\n"
    "s_waitcnt vmcnt(10)\n"
    "v_mfma_f32_16x16x32_bf16 v[120:123], v[32:35], a[92:95], 0\n"
    "buffer_load_dwordx4 a[36:39], v7, s[12:15], s67 offen offset:2048 sc1\n"
    "s_waitcnt lgkmcnt(5)\n"
    "v_mfma_f32_16x16x32_bf16 v[104:107], v[36:39], a[0:3], 0\n"
    "ds_read_b128 v[60:63], v7 offset:7168\n"
    "v_mfma_f32_16x16x32_bf16 v[124:127], v[36:39], a[92:95], 0\n"
    "s_waitcnt lgkmcnt(5)\n"
    "v_mfma_f32_16x16x32_bf16 v[108:111], v[40:43], a[0:3], 0\n"
    "ds_read_b128 v[64:67], v7 offset:8192\n"
    "v_mfma_f32_16x16x32_bf16 v[128:131], v[40:43], a[92:95], 0\n"
    "s_waitcnt lgkmcnt(5)\n"
    "v_mfma_f32_16x16x32_bf16 v[112:115], v[44:47], a[0:3], 0\n"
    "ds_read_b128 v[68:71], v7 offset:9216\n"
    "v_mfma_f32_16x16x32_bf16 v[132:135], v[44:47], a[92:95], 0\n"
    "buffer_load_dwordx4 a[116:119], v7, s[12:15], s68 offen offset:2048 sc1\n"
    "s_waitcnt lgkmcnt(5)\n"
    "v_mfma_f32_16x16x32_bf16 v[116:119], v[48:51], a[0:3], 0\n"
    "ds_read_b128 v[32:35], v7 offset:10240\n"
    "v_mfma_f32_16x16x32_bf16 v[136:139], v[48:51], a[92:95], 0\n"
    "s_waitcnt vmcnt(11) lgkmcnt(5)\n"
    "v_mfma_f32_16x16x32_bf16 v[100:103], v[52:55], a[4:7], v[100:103]\n"
    "ds_read_b128 v[36:39], v7 offset:11264\n"
    "s_waitcnt vmcnt(10)\n"
    "v_mfma_f32_16x16x32_bf16 v[120:123], v[52:55], a[96:99], v[120:123]\n"
    "buffer_load_dwordx4 a[40:43], v7, s[12:15], s67 offen offset:3072 sc1\n"
    "s_add_u32 s67, s67, 0x1000\n"
    "s_waitcnt lgkmcnt(5)\n"
    "v_mfma_f32_16x16x32_bf16 v[104:107], v[56:59], a[4:7], v[104:107]\n"
    "ds_read_b128 v[40:43], v7 offset:12288\n"
    "v_mfma_f32_16x16x32_bf16 v[124:127], v[56:59], a[96:99], v[124:127]\n"
    "s_waitcnt lgkmcnt(5)\n"
    "v_mfma_f32_16x16x32_bf16 v[108:111], v[60:63], a[4:7], v[108:111]\n"
    "ds_read_b128 v[44:47], v7 offset:13312\n"
    "v_mfma_f32_16x16x32_bf16 v[128:131], v[60:63], a[96:99], v[128:131]\n"
    "s_waitcnt lgkmcnt(5)\n"
    "v_mfma_f32_16x16x32_bf16 v[112:115], v[64:67], a[4:7], v[112:115]\n"
    "ds_read_b128 v[48:51], v7 offset:14336\n"
    "v_mfma_f32_16x16x32_bf16 v[132:135], v[64:67], a[96:99], v[132:135]\n"
    "buffer_load_dwordx4 a[120:123], v7, s[12:15], s68 offen offset:3072 sc1\n"
    "s_add_u32 s68, s68, 0x1000\n"
    "s_waitcnt lgkmcnt(5)\n"
    "v_mfma_f32_16x16x32_bf16 v[116:119], v[68:71], a[4:7], v[116:119]\n"
    "ds_read_b128 v[52:55], v7 offset:15360\n"
    "v_mfma_f32_16x16x32_bf16 v[136:139], v[68:71], a[96:99], v[136:139]\n"
    "s_waitcnt vmcnt(11) lgkmcnt(5)\n"
    "v_mfma_f32_16x16x32_bf16 v[100:103], v[32:35], a[8:11], v[100:103]\n"
    "ds_read_b128 v[56:59], v7 offset:16384\n"
    "s_waitcnt vmcnt(10)\n"
    "v_mfma_f32_16x16x32_bf16 v[120:123], v[32:35], a[100:103], v[120:123]\n"
    "s_waitcnt lgkmcnt(5)\n"
    "v_mfma_f32_16x16x32_bf16 v[104:107], v[36:39], a[8:11], v[104:107]\n"
    "ds_read_b128 v[60:63], v7 offset:17408\n"
    "v_mfma_f32_16x16x32_bf16 v[124:127], v[36:39], a[100:103], v[124:127]\n"
    "s_waitcnt lgkmcnt(5)\n"
    "v_mfma_f32_16x16x32_bf16 v[108:111], v[40:43], a[8:11], v[108:111]\n"
    "ds_read_b128 v[64:67], v7 offset:18432\n"
    "v_mfma_f32_16x16x32_bf16 v[128:131], v[40:43], a[100:103], v[128:131]\n"
    "s_waitcnt lgkmcnt(5)\n"
    "v_mfma_f32_16x16x32_bf16 v[112:115], v[44:47], a[8:11], v[112:115]\n"
    "ds_read_b128 v[68:71], v7 offset:19456\n"
    "v_mfma_f32_16x16x32_bf16 v[132:135], v[44:47], a[100:103], v[132:135]\n"
    "buffer_load_dwordx4 a[44:47], v7, s[12:15], s67 offen sc1\n"
    "s_waitcnt lgkmcnt(5)\n"
    "v_mfma_f32_16x16x32_bf16 v[116:119], v[48:51], a[8:11], v[116:119]\n"
    "ds_read_b128 v[32:35], v7 offset:20480\n"
    "v_mfma_f32_16x16x32_bf16 v[136:139], v[48:51], a[100:103], v[136:139]\n"
    "s_waitcnt vmcnt(10) lgkmcnt(5)\n"
    "v_mfma_f32_16x16x32_bf16 v[100:103], v[52:55], a[12:15], v[100:103]\n"
    "ds_read_b128 v[36:39], v7 offset:21504\n"
    "s_waitcnt vmcnt(9)\n"
    "v_mfma_f32_16x16x32_bf16 v[120:123], v[52:55], a[104:107], v[120:123]\n"
    "buffer_load_dwordx4 a[124:127], v7, s[12:15], s68 offen sc1\n"
    "s_waitcnt lgkmcnt(5)\n"
    "v_mfma_f32_16x16x32_bf16 v[104:107], v[56:59], a[12:15], v[104:107]\n"
    "ds_read_b128 v[40:43], v7 offset:22528\n"
    "v_mfma_f32_16x16x32_bf16 v[124:127], v[56:59], a[104:107], v[124:127]\n"
    "s_waitcnt lgkmcnt(5)\n"
    "v_mfma_f32_16x16x32_bf16 v[108:111], v[60:63], a[12:15], v[108:111]\n"
    "ds_read_b128 v[44:47], v7 offset:23552\n"
    "v_mfma_f32_16x16x32_bf16 v[128:131], v[60:63], a[104:107], v[128:131]\n"
    "s_waitcnt lgkmcnt(5)\n"
    "v_mfma_f32_16x16x32_bf16 v[112:115], v[64:67], a[12:15], v[112:115]\n"
    "ds_read_b128 v[48:51], v7 offset:24576\n"
    "v_mfma_f32_16x16x32_bf16 v[132:135], v[64:67], a[104:107], v[132:135]\n"
    "buffer_load_dwordx4 a[48:51], v7, s[12:15], s67 offen offset:1024 sc1\n"
    "s_waitcnt lgkmcnt(5)\n"
    "v_mfma_f32_16x16x32_bf16 v[116:119], v[68:71], a[12:15], v[116:119]\n"
    "ds_read_b128 v[52:55], v7 offset:25600\n"
    "v_mfma_f32_16x16x32_bf16 v[136:139], v[68:71], a[104:107], v[136:139]\n"
    "s_waitcnt vmcnt(10) lgkmcnt(5)\n"
    "v_mfma_f32_16x16x32_bf16 v[100:103], v[32:35], a[28:31], v[100:103]\n"
    "ds_read_b128 v[56:59], v7 offset:26624\n"
    "s_waitcnt vmcnt(9)\n"
    "v_mfma_f32_16x16x32_bf16 v[120:123], v[32:35], a[108:111], v[120:123]\n"
    "buffer_load_dwordx4 a[128:131], v7, s[12:15], s68 offen offset:1024 sc1\n"
    "s_waitcnt lgkmcnt(5)\n"
    "v_mfma_f32_16x16x32_bf16 v[104:107], v[36:39], a[28:31], v[104:107]\n"
    "ds_read_b128 v[60:63], v7 offset:27648\n"
    "v_mfma_f32_16x16x32_bf16 v[124:127], v[36:39], a[108:111], v[124:127]\n"
    "s_waitcnt lgkmcnt(5)\n"
    "v_mfma_f32_16x16x32_bf16 v[108:111], v[40:43], a[28:31], v[108:111]\n"
    "ds_read_b128 v[64:67], v7 offset:28672\n"
    "v_mfma_f32_16x16x32_bf16 v[128:131], v[40:43], a[108:111], v[128:131]\n"
    "s_waitcnt lgkmcnt(5)\n"
    "v_mfma_f32_16x16x32_bf16 v[112:115], v[44:47], a[28:31], v[112:115]\n"
    "ds_read_b128 v[68:71], v7 offset:29696\n"
    "v_mfma_f32_16x16x32_bf16 v[132:135], v[44:47], a[108:111], v[132:135]\n"
    "buffer_load_dwordx4 a[52:55], v7, s[12:15], s67 offen offset:2048 sc1\n"
    "s_waitcnt lgkmcnt(5)\n"
    "v_mfma_f32_16x16x32_bf16 v[116:119], v[48:51], a[28:31], v[116:119]\n"
    "ds_read_b128 v[32:35], v7 offset:30720\n"
    "v_mfma_f32_16x16x32_bf16 v[136:139], v[48:51], a[108:111], v[136:139]\n"
    "s_waitcnt vmcnt(10) lgkmcnt(5)\n"
    "v_mfma_f32_16x16x32_bf16 v[100:103], v[52:55], a[32:35], v[100:103]\n"
    "ds_read_b128 v[36:39], v7 offset:31744\n"
    "s_waitcnt vmcnt(9)\n"
    "v_mfma_f32_16x16x32_bf16 v[120:123], v[52:55], a[112:115], v[120:123]\n"
    "buffer_load_dwordx4 a[132:135], v7, s[12:15], s68 offen offset:2048 sc1\n"
    "s_waitcnt lgkmcnt(5)\n"
    "v_mfma_f32_16x16x32_bf16 v[104:107], v[56:59], a[32:35], v[104:107]\n"
    "ds_read_b128 v[40:43], v7 offset:32768\n"
    "v_mfma_f32_16x16x32_bf16 v[124:127], v[56:59], a[112:115], v[124:127]\n"
    "s_waitcnt lgkmcnt(5)\n"
    "v_mfma_f32_16x16x32_bf16 v[108:111], v[60:63], a[32:35], v[108:111]\n"
    "ds_read_b128 v[44:47], v7 offset:33792\n"
    "v_mfma_f32_16x16x32_bf16 v[128:131], v[60:63], a[112:115], v[128:131]\n"
    "s_waitcnt lgkmcnt(5)\n"
    "v_mfma_f32_16x16x32_bf16 v[112:115], v[64:67], a[32:35], v[112:115]\n"
    "ds_read_b128 v[48:51], v7 offset:34816\n"
    "v_mfma_f32_16x16x32_bf16 v[132:135], v[64:67], a[112:115], v[132:135]\n"
    "buffer_load_dwordx2 v[140:141], v23, s[12:15], s69 offen sc1\n"
    "buffer_load_dwordx2 v[142:143], v23, s[12:15], s69 offen offset:128 sc1\n"
    "s_waitcnt lgkmcnt(5)\n"
    "v_mfma_f32_16x16x32_bf16 v[116:119], v[68:71], a[32:35], v[116:119]\n"
    "ds_read_b128 v[52:55], v7 offset:35840\n"
    "v_mfma_f32_16x16x32_bf16 v[136:139], v[68:71], a[112:115], v[136:139]\n"
    "s_waitcnt vmcnt(11) lgkmcnt(5)\n"
    "v_mfma_f32_16x16x32_bf16 v[100:103], v[32:35], a[36:39], v[100:103]\n"
    "ds_read_b128 v[56:59], v7 offset:36864\n"
    "s_waitcnt vmcnt(10)\n"
    "v_mfma_f32_16x16x32_bf16 v[120:123], v[32:35], a[116:119], v[120:123]\n"
    "buffer_load_dwordx2 v[144:145], v23, s[12:15], s69 offen offset:1024 sc1\n"
    "buffer_load_dwordx2 v[146:147], v23, s[12:15], s69 offen offset:1152 sc1\n"
    "s_waitcnt lgkmcnt(5)\n"
    "v_mfma_f32_16x16x32_bf16 v[104:107], v[36:39], a[36:39], v[104:107]\n"
    "ds_read_b128 v[60:63], v7 offset:37888\n"
    "v_mfma_f32_16x16x32_bf16 v[124:127], v[36:39], a[116:119], v[124:127]\n"
    "s_waitcnt lgkmcnt(5)\n"
    "v_mfma_f32_16x16x32_bf16 v[108:111], v[40:43], a[36:39], v[108:111]\n"
    "ds_read_b128 v[64:67], v7 offset:38912\n"
    "v_mfma_f32_16x16x32_bf16 v[128:131], v[40:43], a[116:119], v[128:131]\n"
    "s_waitcnt lgkmcnt(5)\n"
    "v_mfma_f32_16x16x32_bf16 v[112:115], v[44:47], a[36:39], v[112:115]\n"
    "ds_read_b128 v[68:71], v7 offset:39936\n"
    "v_mfma_f32_16x16x32_bf16 v[132:135], v[44:47], a[116:119], v[132:135]\n"
    "buffer_load_dwordx4 a[56:59], v7, s[12:15], s67 offen offset:3072 sc1\n"
    "s_add_u32 s67, s67, 0x1000\n"
    "s_waitcnt lgkmcnt(5)\n"
    "v_mfma_f32_16x16x32_bf16 v[116:119], v[48:51], a[36:39], v[116:119]\n"
    "ds_read_b128 v[32:35], v7 offset:40960\n"
    "v_mfma_f32_16x16x32_bf16 v[136:139], v[48:51], a[116:119], v[136:139]\n"
    "s_waitcnt vmcnt(12) lgkmcnt(5)\n"
    "v_mfma_f32_16x16x32_bf16 v[100:103], v[52:55], a[40:43], v[100:103]\n"
    "ds_read_b128 v[36:39], v7 offset:41984\n"
    "s_waitcnt vmcnt(11)\n"
    "v_mfma_f32_16x16x32_bf16 v[120:123], v[52:55], a[120:123], v[120:123]\n"
    "buffer_load_dwordx4 a[136:139], v7, s[12:15], s68 offen offset:3072 sc1\n"
    "s_add_u32 s68, s68, 0x1000\n"
    "s_waitcnt lgkmcnt(5)\n"
    "v_mfma_f32_16x16x32_bf16 v[104:107], v[56:59], a[40:43], v[104:107]\n"
    "ds_read_b128 v[40:43], v7 offset:43008\n"
    "v_mfma_f32_16x16x32_bf16 v[124:127], v[56:59], a[120:123], v[124:127]\n"
    "s_waitcnt lgkmcnt(5)\n"
    "v_mfma_f32_16x16x32_bf16 v[108:111], v[60:63], a[40:43], v[108:111]\n"
    "ds_read_b128 v[44:47], v7 offset:44032\n"
    "v_mfma_f32_16x16x32_bf16 v[128:131], v[60:63], a[120:123], v[128:131]\n"
    "s_waitcnt lgkmcnt(5)\n"
    "v_mfma_f32_16x16x32_bf16 v[112:115], v[64:67], a[40:43], v[112:115]\n"
    "ds_read_b128 v[48:51], v7 offset:45056\n"
    "v_mfma_f32_16x16x32_bf16 v[132:135], v[64:67], a[120:123], v[132:135]\n"
    "buffer_load_dwordx4 a[60:63], v7, s[12:15], s67 offen sc1\n"
    "s_waitcnt lgkmcnt(5)\n"
    "v_mfma_f32_16x16x32_bf16 v[116:119], v[68:71], a[40:43], v[116:119]\n"
    "ds_read_b128 v[52:55], v7 offset:46080\n"
    "v_mfma_f32_16x16x32_bf16 v[136:139], v[68:71], a[120:123], v[136:139]\n"
    "s_waitcnt vmcnt(12) lgkmcnt(5)\n"
    "v_mfma_f32_16x16x32_bf16 v[100:103], v[32:35], a[44:47], v[100:103]\n"
    "ds_read_b128 v[56:59], v7 offset:47104\n"
    "s_waitcnt vmcnt(11)\n"
    "v_mfma_f32_16x16x32_bf16 v[120:123], v[32:35], a[124:127], v[120:123]\n"
    "buffer_load_dwordx4 a[140:143], v7, s[12:15], s68 offen sc1\n"
    "s_waitcnt lgkmcnt(5)\n"
    "v_mfma_f32_16x16x32_bf16 v[104:107], v[36:39], a[44:47], v[104:107]\n"
    "ds_read_b128 v[60:63], v7 offset:48128\n"
    "v_mfma_f32_16x16x32_bf16 v[124:127], v[36:39], a[124:127], v[124:127]\n"
    "s_waitcnt lgkmcnt(5)\n"
    "v_mfma_f32_16x16x32_bf16 v[108:111], v[40:43], a[44:47], v[108:111]\n"
    "ds_read_b128 v[64:67], v7 offset:49152\n"
    "v_mfma_f32_16x16x32_bf16 v[128:131], v[40:43], a[124:127], v[128:131]\n"
    "s_waitcnt lgkmcnt(5)\n"
    "v_mfma_f32_16x16x32_bf16 v[112:115], v[44:47], a[44:47], v[112:115]\n"
    "ds_read_b128 v[68:71], v7 offset:50176\n"
    "v_mfma_f32_16x16x32_bf16 v[132:135], v[44:47], a[124:127], v[132:135]\n"
    "buffer_load_dwordx4 a[64:67], v7, s[12:15], s67 offen offset:1024 sc1\n"
    "s_waitcnt lgkmcnt(5)\n"
    "v_mfma_f32_16x16x32_bf16 v[116:119], v[48:51], a[44:47], v[116:119]\n"
    "ds_read_b128 v[32:35], v26\n"
    "v_mfma_f32_16x16x32_bf16 v[136:139], v[48:51], a[124:127], v[136:139]\n"
    "s_waitcnt vmcnt(12) lgkmcnt(5)\n"
    "v_mfma_f32_16x16x32_bf16 v[100:103], v[52:55], a[48:51], v[100:103]\n"
    "ds_read_b128 v[36:39], v26 offset:1024\n"
    "s_waitcnt vmcnt(11)\n"
    "v_mfma_f32_16x16x32_bf16 v[120:123], v[52:55], a[128:131], v[120:123]\n"
    "buffer_load_dwordx4 a[144:147], v7, s[12:15], s68 offen offset:1024 sc1\n"
    "s_waitcnt lgkmcnt(5)\n"
    "v_mfma_f32_16x16x32_bf16 v[104:107], v[56:59], a[48:51], v[104:107]\n"
    "ds_read_b128 v[40:43], v26 offset:2048\n"
    "v_mfma_f32_16x16x32_bf16 v[124:127], v[56:59], a[128:131], v[124:127]\n"
    "s_waitcnt lgkmcnt(5)\n"
    "v_mfma_f32_16x16x32_bf16 v[108:111], v[60:63], a[48:51], v[108:111]\n"
    "ds_read_b128 v[44:47], v26 offset:3072\n"
    "v_mfma_f32_16x16x32_bf16 v[128:131], v[60:63], a[128:131], v[128:131]\n"
    "s_waitcnt lgkmcnt(5)\n"
    "v_mfma_f32_16x16x32_bf16 v[112:115], v[64:67], a[48:51], v[112:115]\n"
    "ds_read_b128 v[48:51], v26 offset:4096\n"
    "v_mfma_f32_16x16x32_bf16 v[132:135], v[64:67], a[128:131], v[132:135]\n"
    "buffer_load_dwordx4 a[68:71], v7, s[12:15], s67 offen offset:2048 sc1\n"
    "s_waitcnt lgkmcnt(5)\n"
    "v_mfma_f32_16x16x32_bf16 v[116:119], v[68:71], a[48:51], v[116:119]\n"
    "ds_read_b128 v[52:55], v26 offset:5120\n"
    "v_mfma_f32_16x16x32_bf16 v[136:139], v[68:71], a[128:131], v[136:139]\n"
    "s_waitcnt vmcnt(12) lgkmcnt(5)\n"
    "s_barrier\n"
    "v_mfma_f32_16x16x32_bf16 v[100:103], v[32:35], a[52:55], v[100:103]\n"
    "ds_read_b128 v[56:59], v26 offset:6144\n"
    "s_waitcnt vmcnt(11)\n"
    "v_mfma_f32_16x16x32_bf16 v[120:123], v[32:35], a[132:135], v[120:123]\n"
    "buffer_load_dwordx4 a[148:151], v7, s[12:15], s68 offen offset:2048 sc1\n"
    "s_waitcnt lgkmcnt(5)\n"
    "ds_read_b128 v[72:75], v16\n"
    "v_mfma_f32_16x16x32_bf16 v[104:107], v[36:39], a[52:55], v[104:107]\n"
    "ds_read_b128 v[60:63], v26 offset:7168\n"
    "v_mfma_f32_16x16x32_bf16 v[124:127], v[36:39], a[132:135], v[124:127]\n"
    "s_waitcnt lgkmcnt(6)\n"
    "ds_read_b128 v[76:79], v16 offset:256\n"
    "v_mfma_f32_16x16x32_bf16 v[108:111], v[40:43], a[52:55], v[108:111]\n"
    "ds_read_b128 v[64:67], v26 offset:8192\n"
    "v_mfma_f32_16x16x32_bf16 v[128:131], v[40:43], a[132:135], v[128:131]\n"
    "s_waitcnt lgkmcnt(7)\n"
    "ds_read_b128 v[80:83], v16 offset:512\n"
    "v_mfma_f32_16x16x32_bf16 v[112:115], v[44:47], a[52:55], v[112:115]\n"
    "ds_read_b128 v[68:71], v26 offset:9216\n"
    "v_mfma_f32_16x16x32_bf16 v[132:135], v[44:47], a[132:135], v[132:135]\n"
    "buffer_load_dwordx4 a[72:75], v7, s[12:15], s67 offen offset:3072 sc1\n"
    "s_add_u32 s67, s67, 0x1000\n"
    "s_waitcnt lgkmcnt(8)\n"
    "v_mfma_f32_16x16x32_bf16 v[116:119], v[48:51], a[52:55], v[116:119]\n"
    "ds_read_b128 v[32:35], v26 offset:10240\n"
    "v_mfma_f32_16x16x32_bf16 v[136:139], v[48:51], a[132:135], v[136:139]\n"
    "s_waitcnt vmcnt(8) lgkmcnt(8)\n"
    "ds_read_b128 v[84:87], v16 offset:768\n"
    "v_mfma_f32_16x16x32_bf16 v[100:103], v[52:55], a[56:59], v[100:103]\n"
    "ds_read_b128 v[36:39], v26 offset:11264\n"
    "s_waitcnt vmcnt(7)\n"
    "v_mfma_f32_16x16x32_bf16 v[120:123], v[52:55], a[136:139], v[120:123]\n"
    "buffer_load_dwordx4 a[152:155], v7, s[12:15], s68 offen offset:3072 sc1\n"
    "s_add_u32 s68, s68, 0x1000\n"
    "s_waitcnt lgkmcnt(9)\n"
    "v_mfma_f32_16x16x32_bf16 v[104:107], v[56:59], a[56:59], v[104:107]\n"
    "ds_read_b128 v[40:43], v26 offset:12288\n"
    "v_mfma_f32_16x16x32_bf16 v[124:127], v[56:59], a[136:139], v[124:127]\n"
    "s_waitcnt lgkmcnt(8)\n"
    "ds_read_b128 v[88:91], v16 offset:1024\n"
    "v_mfma_f32_16x16x32_bf16 v[108:111], v[60:63], a[56:59], v[108:111]\n"
    "ds_read_b128 v[44:47], v26 offset:13312\n"
    "v_mfma_f32_16x16x32_bf16 v[128:131], v[60:63], a[136:139], v[128:131]\n"
    "s_waitcnt lgkmcnt(8)\n"
    "ds_read_b128 v[92:95], v16 offset:1280\n"
    "v_mfma_f32_16x16x32_bf16 v[112:115], v[64:67], a[56:59], v[112:115]\n"
    "ds_read_b128 v[48:51], v26 offset:14336\n"
    "v_mfma_f32_16x16x32_bf16 v[132:135], v[64:67], a[136:139], v[132:135]\n"
    "buffer_load_dwordx4 a[76:79], v7, s[12:15], s67 offen sc1\n"
    "s_waitcnt lgkmcnt(8)\n"
    "ds_read_b128 v[96:99], v16 offset:1536\n"
    "v_mfma_f32_16x16x32_bf16 v[116:119], v[68:71], a[56:59], v[116:119]\n"
    "ds_read_b128 v[52:55], v26 offset:15360\n"
    "v_add_f32_dpp v72, v72, v72 quad_perm:[1,0,3,2] row_mask:0xf bank_mask:0xf\n"
    "v_add_f32_dpp v73, v73, v73 quad_perm:[1,0,3,2] row_mask:0xf bank_mask:0xf\n"
    "v_mfma_f32_16x16x32_bf16 v[136:139], v[68:71], a[136:139], v[136:139]\n"
    "s_waitcnt vmcnt(8) lgkmcnt(9)\n"
    "v_add_f32_dpp v74, v74, v74 quad_perm:[1,0,3,2] row_mask:0xf bank_mask:0xf\n"
    "v_add_f32_dpp v75, v75, v75 quad_perm:[1,0,3,2] row_mask:0xf bank_mask:0xf\n"
    "v_mfma_f32_16x16x32_bf16 v[100:103], v[32:35], a[60:63], v[100:103]\n"
    "ds_read_b128 v[56:59], v26 offset:16384\n"
    "s_waitcnt vmcnt(7)\n"
    "v_add_f32_dpp v72, v72, v72 quad_perm:[2,3,0,1] row_mask:0xf bank_mask:0xf\n"
    "v_mfma_f32_16x16x32_bf16 v[120:123], v[32:35], a[140:143], v[120:123]\n"
    "buffer_load_dwordx4 a[156:159], v7, s[12:15], s68 offen sc1\n"
    "s_waitcnt lgkmcnt(8)\n"
    "v_add_f32_dpp v73, v73, v73 quad_perm:[2,3,0,1] row_mask:0xf bank_mask:0xf\n"
    "v_mfma_f32_16x16x32_bf16 v[104:107], v[36:39], a[60:63], v[104:107]\n"
    "ds_read_b128 v[60:63], v26 offset:17408\n"
    "v_add_f32_dpp v74, v74, v74 quad_perm:[2,3,0,1] row_mask:0xf bank_mask:0xf\n"
    "v_add_f32_dpp v75, v75, v75 quad_perm:[2,3,0,1] row_mask:0xf bank_mask:0xf\n"
    "v_mfma_f32_16x16x32_bf16 v[124:127], v[36:39], a[140:143], v[124:127]\n"
    "s_waitcnt lgkmcnt(8)\n"
    "v_add_f32_dpp v76, v76, v76 quad_perm:[1,0,3,2] row_mask:0xf bank_mask:0xf\n"
    "v_add_f32_dpp v77, v77, v77 quad_perm:[1,0,3,2] row_mask:0xf bank_mask:0xf\n"
    "v_mfma_f32_16x16x32_bf16 v[108:111], v[40:43], a[60:63], v[108:111]\n"
    "ds_read_b128 v[64:67], v26 offset:18432\n"
    "v_add_f32_dpp v78, v78, v78 quad_perm:[1,0,3,2] row_mask:0xf bank_mask:0xf\n"
    "v_add_f32_dpp v79, v79, v79 quad_perm:[1,0,3,2] row_mask:0xf bank_mask:0xf\n"
    "v_mfma_f32_16x16x32_bf16 v[128:131], v[40:43], a[140:143], v[128:131]\n"
    "s_waitcnt lgkmcnt(7)\n"
    "v_add_f32_dpp v76, v76, v76 quad_perm:[2,3,0,1] row_mask:0xf bank_mask:0xf\n"
    "v_add_f32_dpp v77, v77, v77 quad_perm:[2,3,0,1] row_mask:0xf bank_mask:0xf\n"
    "v_mfma_f32_16x16x32_bf16 v[112:115], v[44:47], a[60:63], v[112:115]\n"
    "ds_read_b128 v[68:71], v26 offset:19456\n"
    "v_add_f32_dpp v78, v78, v78 quad_perm:[2,3,0,1] row_mask:0xf bank_mask:0xf\n"
    "v_add_f32_dpp v79, v79, v79 quad_perm:[2,3,0,1] row_mask:0xf bank_mask:0xf\n"
    "v_mfma_f32_16x16x32_bf16 v[132:135], v[44:47], a[140:143], v[132:135]\n"
    "buffer_load_dwordx4 a[80:83], v7, s[12:15], s67 offen offset:1024 sc1\n"
    "s_waitcnt lgkmcnt(6)\n"
    "v_add_f32_dpp v80, v80, v80 quad_perm:[1,0,3,2] row_mask:0xf bank_mask:0xf\n"
    "v_mfma_f32_16x16x32_bf16 v[116:119], v[48:51], a[60:63], v[116:119]\n"
    "ds_read_b128 v[32:35], v26 offset:20480\n"
    "v_add_f32_dpp v81, v81, v81 quad_perm:[1,0,3,2] row_mask:0xf bank_mask:0xf\n"
    "v_add_f32_dpp v82, v82, v82 quad_perm:[1,0,3,2] row_mask:0xf bank_mask:0xf\n"
    "v_mfma_f32_16x16x32_bf16 v[136:139], v[48:51], a[140:143], v[136:139]\n"
    "s_waitcnt vmcnt(8) lgkmcnt(5)\n"
    "v_add_f32_dpp v83, v83, v83 quad_perm:[1,0,3,2] row_mask:0xf bank_mask:0xf\n"
    "v_add_f32_dpp v80, v80, v80 quad_perm:[2,3,0,1] row_mask:0xf bank_mask:0xf\n"
    "v_mfma_f32_16x16x32_bf16 v[100:103], v[52:55], a[64:67], v[100:103]\n"
    "ds_read_b128 v[36:39], v26 offset:21504\n"
    "s_waitcnt vmcnt(7)\n"
    "v_add_f32_dpp v81, v81, v81 quad_perm:[2,3,0,1] row_mask:0xf bank_mask:0xf\n"
    "v_mfma_f32_16x16x32_bf16 v[120:123], v[52:55], a[144:147], v[120:123]\n"
    "buffer_load_dwordx4 a[160:163], v7, s[12:15], s68 offen offset:1024 sc1\n"
    "s_waitcnt lgkmcnt(5)\n"
    "v_add_f32_dpp v82, v82, v82 quad_perm:[2,3,0,1] row_mask:0xf bank_mask:0xf\n"
    "v_mfma_f32_16x16x32_bf16 v[104:107], v[56:59], a[64:67], v[104:107]\n"
    "ds_read_b128 v[40:43], v26 offset:22528\n"
    "v_add_f32_dpp v83, v83, v83 quad_perm:[2,3,0,1] row_mask:0xf bank_mask:0xf\n"
    "v_add_f32_dpp v84, v84, v84 quad_perm:[1,0,3,2] row_mask:0xf bank_mask:0xf\n"
    "v_mfma_f32_16x16x32_bf16 v[124:127], v[56:59], a[144:147], v[124:127]\n"
    "s_waitcnt lgkmcnt(5)\n"
    "v_add_f32_dpp v85, v85, v85 quad_perm:[1,0,3,2] row_mask:0xf bank_mask:0xf\n"
    "v_add_f32_dpp v86, v86, v86 quad_perm:[1,0,3,2] row_mask:0xf bank_mask:0xf\n"
    "v_mfma_f32_16x16x32_bf16 v[108:111], v[60:63], a[64:67], v[108:111]\n"
    "ds_read_b128 v[44:47], v26 offset:23552\n"
    "v_add_f32_dpp v87, v87, v87 quad_perm:[1,0,3,2] row_mask:0xf bank_mask:0xf\n"
    "v_add_f32_dpp v84, v84, v84 quad_perm:[2,3,0,1] row_mask:0xf bank_mask:0xf\n"
    "v_mfma_f32_16x16x32_bf16 v[128:131], v[60:63], a[144:147], v[128:131]\n"
    "s_waitcnt lgkmcnt(5)\n"
    "v_add_f32_dpp v85, v85, v85 quad_perm:[2,3,0,1] row_mask:0xf bank_mask:0xf\n"
    "v_add_f32_dpp v86, v86, v86 quad_perm:[2,3,0,1] row_mask:0xf bank_mask:0xf\n"
    "v_mfma_f32_16x16x32_bf16 v[112:115], v[64:67], a[64:67], v[112:115]\n"
    "ds_read_b128 v[48:51], v26 offset:24576\n"
    "v_add_f32_dpp v87, v87, v87 quad_perm:[2,3,0,1] row_mask:0xf bank_mask:0xf\n"
    "v_add_f32_dpp v88, v88, v88 quad_perm:[1,0,3,2] row_mask:0xf bank_mask:0xf\n"
    "v_mfma_f32_16x16x32_bf16 v[132:135], v[64:67], a[144:147], v[132:135]\n"
    "buffer_load_dwordx4 a[84:87], v7, s[12:15], s67 offen offset:2048 sc1\n"
    "s_waitcnt lgkmcnt(5)\n"
    "v_add_f32_dpp v89, v89, v89 quad_perm:[1,0,3,2] row_mask:0xf bank_mask:0xf\n"
    "v_mfma_f32_16x16x32_bf16 v[116:119], v[68:71], a[64:67], v[116:119]\n"
    "ds_read_b128 v[52:55], v26 offset:25600\n"
    "v_add_f32_dpp v90, v90, v90 quad_perm:[1,0,3,2] row_mask:0xf bank_mask:0xf\n"
    "v_add_f32_dpp v91, v91, v91 quad_perm:[1,0,3,2] row_mask:0xf bank_mask:0xf\n"
    "v_mfma_f32_16x16x32_bf16 v[136:139], v[68:71], a[144:147], v[136:139]\n"
    "s_waitcnt vmcnt(8) lgkmcnt(5)\n"
    "v_add_f32_dpp v88, v88, v88 quad_perm:[2,3,0,1] row_mask:0xf bank_mask:0xf\n"
    "v_add_f32_dpp v89, v89, v89 quad_perm:[2,3,0,1] row_mask:0xf bank_mask:0xf\n"
    "v_mfma_f32_16x16x32_bf16 v[100:103], v[32:35], a[68:71], v[100:103]\n"
    "ds_read_b128 v[56:59], v26 offset:26624\n"
    "s_waitcnt vmcnt(7)\n"
    "v_add_f32_dpp v90, v90, v90 quad_perm:[2,3,0,1] row_mask:0xf bank_mask:0xf\n"
    "v_mfma_f32_16x16x32_bf16 v[120:123], v[32:35], a[148:151], v[120:123]\n"
    "buffer_load_dwordx4 a[164:167], v7, s[12:15], s68 offen offset:2048 sc1\n"
    "s_waitcnt lgkmcnt(5)\n"
    "v_add_f32_dpp v91, v91, v91 quad_perm:[2,3,0,1] row_mask:0xf bank_mask:0xf\n"
    "v_mfma_f32_16x16x32_bf16 v[104:107], v[36:39], a[68:71], v[104:107]\n"
    "ds_read_b128 v[60:63], v26 offset:27648\n"
    "v_add_f32_dpp v92, v92, v92 quad_perm:[1,0,3,2] row_mask:0xf bank_mask:0xf\n"
    "v_add_f32_dpp v93, v93, v93 quad_perm:[1,0,3,2] row_mask:0xf bank_mask:0xf\n"
    "v_mfma_f32_16x16x32_bf16 v[124:127], v[36:39], a[148:151], v[124:127]\n"
    "s_waitcnt lgkmcnt(5)\n"
    "v_add_f32_dpp v94, v94, v94 quad_perm:[1,0,3,2] row_mask:0xf bank_mask:0xf\n"
    "v_add_f32_dpp v95, v95, v95 quad_perm:[1,0,3,2] row_mask:0xf bank_mask:0xf\n"
    "v_mfma_f32_16x16x32_bf16 v[108:111], v[40:43], a[68:71], v[108:111]\n"
    "ds_read_b128 v[64:67], v26 offset:28672\n"
    "v_add_f32_dpp v92, v92, v92 quad_perm:[2,3,0,1] row_mask:0xf bank_mask:0xf\n"
    "v_add_f32_dpp v93, v93, v93 quad_perm:[2,3,0,1] row_mask:0xf bank_mask:0xf\n"
    "v_mfma_f32_16x16x32_bf16 v[128:131], v[40:43], a[148:151], v[128:131]\n"
    "s_waitcnt lgkmcnt(5)\n"
    "v_add_f32_dpp v94, v94, v94 quad_perm:[2,3,0,1] row_mask:0xf bank_mask:0xf\n"
    "v_add_f32_dpp v95, v95, v95 quad_perm:[2,3,0,1] row_mask:0xf bank_mask:0xf\n"
    "v_mfma_f32_16x16x32_bf16 v[112:115], v[44:47], a[68:71], v[112:115]\n"
    "ds_read_b128 v[68:71], v26 offset:29696\n"
    "v_add_f32_dpp v96, v96, v96 quad_perm:[1,0,3,2] row_mask:0xf bank_mask:0xf\n"
    "v_add_f32_dpp v97, v97, v97 quad_perm:[1,0,3,2] row_mask:0xf bank_mask:0xf\n"
    "v_mfma_f32_16x16x32_bf16 v[132:135], v[44:47], a[148:151], v[132:135]\n"
    "buffer_load_dwordx4 a[88:91], v7, s[12:15], s67 offen offset:3072 sc1\n"
    "s_add_u32 s67, s67, 0x1000\n"
    "s_waitcnt lgkmcnt(5)\n"
    "v_mfma_f32_16x16x32_bf16 v[116:119], v[48:51], a[68:71], v[116:119]\n"
    "ds_read_b128 v[32:35], v26 offset:30720\n"
    "v_add_f32_dpp v98, v98, v98 quad_perm:[1,0,3,2] row_mask:0xf bank_mask:0xf\n"
    "v_add_f32_dpp v99, v99, v99 quad_perm:[1,0,3,2] row_mask:0xf bank_mask:0xf\n"
    "v_mfma_f32_16x16x32_bf16 v[136:139], v[48:51], a[148:151], v[136:139]\n"
    "s_waitcnt vmcnt(8) lgkmcnt(5)\n"
    "v_add_f32_dpp v96, v96, v96 quad_perm:[2,3,0,1] row_mask:0xf bank_mask:0xf\n"
    "v_add_f32_dpp v97, v97, v97 quad_perm:[2,3,0,1] row_mask:0xf bank_mask:0xf\n"
    "v_mfma_f32_16x16x32_bf16 v[100:103], v[52:55], a[72:75], v[100:103]\n"
    "ds_read_b128 v[36:39], v26 offset:31744\n"
    "s_waitcnt vmcnt(7)\n"
    "v_add_f32_dpp v98, v98, v98 quad_perm:[2,3,0,1] row_mask:0xf bank_mask:0xf\n"
    "v_mfma_f32_16x16x32_bf16 v[120:123], v[52:55], a[152:155], v[120:123]\n"
    "buffer_load_dwordx4 a[168:171], v7, s[12:15], s68 offen offset:3072 sc1\n"
    "s_add_u32 s68, s68, 0x1000\n"
    "s_waitcnt lgkmcnt(5)\n"
    "v_mfma_f32_16x16x32_bf16 v[104:107], v[56:59], a[72:75], v[104:107]\n"
    "ds_read_b128 v[40:43], v26 offset:32768\n"
    "v_add_f32_dpp v99, v99, v99 quad_perm:[2,3,0,1] row_mask:0xf bank_mask:0xf\n"
    "v_add_f32 v140, v140, v142\n"
    "v_mfma_f32_16x16x32_bf16 v[124:127], v[56:59], a[152:155], v[124:127]\n"
    "s_waitcnt lgkmcnt(5)\n"
    "v_add_f32 v141, v141, v143\n"
    "v_add_f32 v144, v144, v146\n"
    "v_mfma_f32_16x16x32_bf16 v[108:111], v[60:63], a[72:75], v[108:111]\n"
    "ds_read_b128 v[44:47], v26 offset:33792\n"
    "v_add_f32 v145, v145, v147\n"
    "v_mov_b32 v142, v140\n"
    "v_mfma_f32_16x16x32_bf16 v[128:131], v[60:63], a[152:155], v[128:131]\n"
    "s_waitcnt lgkmcnt(5)\n"
    "v_mov_b32 v143, v141\n"
    "v_mov_b32 v146, v144\n"
    "v_mfma_f32_16x16x32_bf16 v[112:115], v[64:67], a[72:75], v[112:115]\n"
    "ds_read_b128 v[48:51], v26 offset:34816\n"
    "v_mov_b32 v147, v145\n"
    "v_mfma_f32_16x16x32_bf16 v[132:135], v[64:67], a[152:155], v[132:135]\n"
    "s_mul_i32 s66, s41, 0x2800\n"
    "s_add_u32 m0, s66, 0x1a000\n"
    "s_add_u32 s66, m0, s65\n"
    "buffer_load_dwordx4 v7, s[12:15], s66 offen sc1 lds\n"
    "s_waitcnt lgkmcnt(5)\n"
    "v_mfma_f32_16x16x32_bf16 v[116:119], v[68:71], a[72:75], v[116:119]\n"
    "ds_read_b128 v[52:55], v26 offset:35840\n"
    "v_permlane32_swap_b32 v140, v142\n"
    "v_mfma_f32_16x16x32_bf16 v[136:139], v[68:71], a[152:155], v[136:139]\n"
    "s_waitcnt vmcnt(8) lgkmcnt(5)\n"
    "v_permlane32_swap_b32 v141, v143\n"
    "v_mfma_f32_16x16x32_bf16 v[100:103], v[32:35], a[76:79], v[100:103]\n"
    "ds_read_b128 v[56:59], v26 offset:36864\n"
    "s_waitcnt vmcnt(7)\n"
    "v_mfma_f32_16x16x32_bf16 v[120:123], v[32:35], a[156:159], v[120:123]\n"
    "buffer_load_dwordx4 v7, s[12:15], s66 offen offset:1024 sc1 lds\n"
    "s_waitcnt lgkmcnt(5)\n"
    "v_mfma_f32_16x16x32_bf16 v[104:107], v[36:39], a[76:79], v[104:107]\n"
    "ds_read_b128 v[60:63], v26 offset:37888\n"
    "v_permlane32_swap_b32 v144, v146\n"
    "v_mfma_f32_16x16x32_bf16 v[124:127], v[36:39], a[156:159], v[124:127]\n"
    "s_waitcnt lgkmcnt(5)\n"
    "v_permlane32_swap_b32 v145, v147\n"
    "v_mfma_f32_16x16x32_bf16 v[108:111], v[40:43], a[76:79], v[108:111]\n"
    "ds_read_b128 v[64:67], v26 offset:38912\n"
    "v_add_f32 v140, v140, v142\n"
    "v_add_f32 v141, v141, v143\n"
    "v_mfma_f32_16x16x32_bf16 v[128:131], v[40:43], a[156:159], v[128:131]\n"
    "s_waitcnt lgkmcnt(5)\n"
    "v_add_f32 v144, v144, v146\n"
    "v_add_f32 v145, v145, v147\n"
    "v_mfma_f32_16x16x32_bf16 v[112:115], v[44:47], a[76:79], v[112:115]\n"
    "ds_read_b128 v[68:71], v26 offset:39936\n"
    "v_mov_b32 v142, v140\n"
    "v_mov_b32 v143, v141\n"
    "v_mfma_f32_16x16x32_bf16 v[132:135], v[44:47], a[156:159], v[132:135]\n"
    "buffer_load_dwordx4 v7, s[12:15], s66 offen offset:2048 sc1 lds\n"
    "s_waitcnt lgkmcnt(5)\n"
    "v_mov_b32 v146, v144\n"
    "v_mfma_f32_16x16x32_bf16 v[116:119], v[48:51], a[76:79], v[116:119]\n"
    "ds_read_b128 v[32:35], v26 offset:40960\n"
    "v_mov_b32 v147, v145\n"
    "v_mfma_f32_16x16x32_bf16 v[136:139], v[48:51], a[156:159], v[136:139]\n"
    "s_waitcnt vmcnt(8) lgkmcnt(5)\n"
    "v_permlane16_swap_b32 v140, v142\n"
    "v_mfma_f32_16x16x32_bf16 v[100:103], v[52:55], a[80:83], v[100:103]\n"
    "ds_read_b128 v[36:39], v26 offset:41984\n"
    "s_waitcnt vmcnt(7)\n"
    "v_mfma_f32_16x16x32_bf16 v[120:123], v[52:55], a[160:163], v[120:123]\n"
    "buffer_load_dwordx4 v7, s[12:15], s66 offen offset:3072 sc1 lds\n"
    "s_waitcnt lgkmcnt(5)\n"
    "v_mfma_f32_16x16x32_bf16 v[104:107], v[56:59], a[80:83], v[104:107]\n"
    "ds_read_b128 v[40:43], v26 offset:43008\n"
    "v_permlane16_swap_b32 v141, v143\n"
    "v_mfma_f32_16x16x32_bf16 v[124:127], v[56:59], a[160:163], v[124:127]\n"
    "s_waitcnt lgkmcnt(5)\n"
    "v_permlane16_swap_b32 v144, v146\n"
    "v_mfma_f32_16x16x32_bf16 v[108:111], v[60:63], a[80:83], v[108:111]\n"
    "ds_read_b128 v[44:47], v26 offset:44032\n"
    "v_permlane16_swap_b32 v145, v147\n"
    "v_mfma_f32_16x16x32_bf16 v[128:131], v[60:63], a[160:163], v[128:131]\n"
    "s_waitcnt lgkmcnt(5)\n"
    "v_add_f32 v140, v140, v142\n"
    "v_add_f32 v141, v141, v143\n"
    "v_mfma_f32_16x16x32_bf16 v[112:115], v[64:67], a[80:83], v[112:115]\n"
    "ds_read_b128 v[48:51], v26 offset:45056\n"
    "v_add_f32 v144, v144, v146\n"
    "v_add_f32_e64 v145, v145, v147\n"
    "v_mfma_f32_16x16x32_bf16 v[132:135], v[64:67], a[160:163], v[132:135]\n"
    "s_mul_i32 s66, s41, 0x2800\n"
    "s_add_u32 m0, s66, 0x1b000\n"
    "s_add_u32 s66, m0, s65\n"
    "buffer_load_dwordx4 v7, s[12:15], s66 offen sc1 lds\n"
    "s_waitcnt lgkmcnt(5)\n"
    "v_mfma_f32_16x16x32_bf16 v[116:119], v[68:71], a[80:83], v[116:119]\n"
    "ds_read_b128 v[52:55], v26 offset:46080\n"
    "v_mul_f32 v140, 0xbacccccd, v140\n"
    "v_mul_f32 v141, 0x3acccccd, v141\n"
    "v_mfma_f32_16x16x32_bf16 v[136:139], v[68:71], a[160:163], v[136:139]\n"
    "s_waitcnt vmcnt(8) lgkmcnt(5)\n"
    "v_mul_f32 v144, 0xbacccccd, v144\n"
    "v_mul_f32 v145, 0x3acccccd, v145\n"
    "v_mfma_f32_16x16x32_bf16 v[100:103], v[32:35], a[84:87], v[100:103]\n"
    "ds_read_b128 v[56:59], v26 offset:47104\n"
    "s_waitcnt vmcnt(7)\n"
    "v_fma_f32 v141, -v140, v140, v141\n"
    "v_mfma_f32_16x16x32_bf16 v[120:123], v[32:35], a[164:167], v[120:123]\n"
    "buffer_load_dwordx4 v7, s[12:15], s66 offen offset:1024 sc1 lds\n"
    "s_waitcnt lgkmcnt(5)\n"
    "v_fma_f32 v145, -v144, v144, v145\n"
    "v_mfma_f32_16x16x32_bf16 v[104:107], v[36:39], a[84:87], v[104:107]\n"
    "ds_read_b128 v[60:63], v26 offset:48128\n"
    "v_max_f32 v141, 0, v141\n"
    "v_max_f32 v145, 0, v145\n"
    "v_mfma_f32_16x16x32_bf16 v[124:127], v[36:39], a[164:167], v[124:127]\n"
    "s_waitcnt lgkmcnt(5)\n"
    "v_add_f32 v141, 0x3727c5ac, v141\n"
    "v_add_f32 v145, 0x3727c5ac, v145\n"
    "v_mfma_f32_16x16x32_bf16 v[108:111], v[40:43], a[84:87], v[108:111]\n"
    "ds_read_b128 v[64:67], v26 offset:49152\n"
    "v_rsq_f32 v142, v141\n"
    "v_mfma_f32_16x16x32_bf16 v[128:131], v[40:43], a[164:167], v[128:131]\n"
    "s_waitcnt lgkmcnt(5)\n"
    "v_rsq_f32 v146, v145\n"
    "v_mfma_f32_16x16x32_bf16 v[112:115], v[44:47], a[84:87], v[112:115]\n"
    "ds_read_b128 v[68:71], v26 offset:50176\n"
    "v_mul_f32 v141, v141, v142\n"
    "v_mul_f32 v145, v145, v146\n"
    "v_mfma_f32_16x16x32_bf16 v[132:135], v[44:47], a[164:167], v[132:135]\n"
    "buffer_load_dwordx4 v[176:179], v7, s[12:15], s78 offen sc1\n"
    "s_waitcnt lgkmcnt(5)\n"
    "v_mul_f32 v142, 0xbfb8aa3b, v142\n"
    "v_mfma_f32_16x16x32_bf16 v[116:119], v[48:51], a[84:87], v[116:119]\n"
    "v_mul_f32 v146, 0xbfb8aa3b, v146\n"
    "v_mfma_f32_16x16x32_bf16 v[136:139], v[48:51], a[164:167], v[136:139]\n"
    "s_waitcnt vmcnt(8) lgkmcnt(4)\n"
    "v_mfma_f32_16x16x32_bf16 v[100:103], v[52:55], a[88:91], v[100:103]\n"
    "s_waitcnt vmcnt(7)\n"
    "v_mfma_f32_16x16x32_bf16 v[120:123], v[52:55], a[168:171], v[120:123]\n"
    "buffer_load_dwordx4 v[172:175], v7, s[12:15], s78 offen offset:1024 sc1\n"
    "s_waitcnt lgkmcnt(3)\n"
    "v_mfma_f32_16x16x32_bf16 v[104:107], v[56:59], a[88:91], v[104:107]\n"
    "v_mfma_f32_16x16x32_bf16 v[124:127], v[56:59], a[168:171], v[124:127]\n"
    "s_waitcnt lgkmcnt(2)\n"
    "v_mfma_f32_16x16x32_bf16 v[108:111], v[60:63], a[88:91], v[108:111]\n"
    "v_mfma_f32_16x16x32_bf16 v[128:131], v[60:63], a[168:171], v[128:131]\n"
    "s_waitcnt lgkmcnt(1)\n"
    "v_mfma_f32_16x16x32_bf16 v[112:115], v[64:67], a[88:91], v[112:115]\n"
    "v_mfma_f32_16x16x32_bf16 v[132:135], v[64:67], a[168:171], v[132:135]\n"
    "s_waitcnt lgkmcnt(0)\n"
    "v_mfma_f32_16x16x32_bf16 v[116:119], v[68:71], a[88:91], v[116:119]\n"
    "v_mfma_f32_16x16x32_bf16 v[136:139], v[68:71], a[168:171], v[136:139]\n"
    "v_add_u32 v26, 0x19000, v7\n"
    "v_pk_fma_f32 v[100:101], v[72:73], v[140:141], v[100:101] op_sel_hi:[1,0,1]\n"
    "v_pk_fma_f32 v[102:103], v[74:75], v[140:141], v[102:103] op_sel_hi:[1,0,1]\n"
    "v_pk_fma_f32 v[104:105], v[76:77], v[140:141], v[104:105] op_sel_hi:[1,0,1]\n"
    "v_pk_fma_f32 v[106:107], v[78:79], v[140:141], v[106:107] op_sel_hi:[1,0,1]\n"
    "v_pk_mul_f32 v[64:65], v[100:101], v[142:143] op_sel_hi:[1,0]\n"
    "buffer_load_dwordx4 v[168:171], v7, s[12:15], s78 offen offset:2048 sc1\n"
    "v_pk_mul_f32 v[66:67], v[102:103], v[142:143] op_sel_hi:[1,0]\n"
    "v_pk_mul_f32 v[68:69], v[104:105], v[142:143] op_sel_hi:[1,0]\n"
    "v_pk_mul_f32 v[70:71], v[106:107], v[142:143] op_sel_hi:[1,0]\n"
    "v_exp_f32 v64, v64\n"
    "v_exp_f32 v65, v65\n"
    "v_exp_f32 v66, v66\n"
    "v_exp_f32 v67, v67\n"
    "v_exp_f32 v68, v68\n"
    "v_exp_f32 v69, v69\n"
    "v_exp_f32 v70, v70\n"
    "v_exp_f32 v71, v71\n"
    "v_pk_fma_f32 v[64:65], v[64:65], v[140:141], v[140:141] op_sel:[0,1,1] op_sel_hi:[1,1,1]\n"
    "v_pk_fma_f32 v[66:67], v[66:67], v[140:141], v[140:141] op_sel:[0,1,1] op_sel_hi:[1,1,1]\n"
    "v_pk_fma_f32 v[68:69], v[68:69], v[140:141], v[140:141] op_sel:[0,1,1] op_sel_hi:[1,1,1]\n"
    "v_pk_fma_f32 v[70:71], v[70:71], v[140:141], v[140:141] op_sel:[0,1,1] op_sel_hi:[1,1,1]\n"
    "v_rcp_f32 v64, v64\n"
    "v_rcp_f32 v65, v65\n"
    "v_rcp_f32 v66, v66\n"
    "v_rcp_f32 v67, v67\n"
    "v_rcp_f32 v68, v68\n"
    "v_rcp_f32 v69, v69\n"
    "v_rcp_f32 v70, v70\n"
    "buffer_load_dwordx4 v[164:167], v7, s[12:15], s78 offen offset:3072 sc1\n"
    "s_add_u32 s78, s78, 0x1000\n"
    "v_rcp_f32 v71, v71\n"
    "v_pk_mul_f32 v[64:65], v[64:65], v[100:101]\n"
    "v_pk_mul_f32 v[66:67], v[66:67], v[102:103]\n"
    "v_pk_mul_f32 v[68:69], v[68:69], v[104:105]\n"
    "v_pk_mul_f32 v[70:71], v[70:71], v[106:107]\n"
    "v_cvt_pk_bf16_f32 v100, v64, v65\n"
    "v_cvt_pk_bf16_f32 v101, v66, v67\n"
    "v_cvt_pk_bf16_f32 v102, v68, v69\n"
    "v_cvt_pk_bf16_f32 v103, v70, v71\n"
    "buffer_store_dwordx4 v[100:103], v7, s[12:15], s88 offen sc1\n"
    "v_pk_fma_f32 v[120:121], v[72:73], v[144:145], v[120:121] op_sel_hi:[1,0,1]\n"
    "v_pk_fma_f32 v[122:123], v[74:75], v[144:145], v[122:123] op_sel_hi:[1,0,1]\n"
    "v_pk_fma_f32 v[124:125], v[76:77], v[144:145], v[124:125] op_sel_hi:[1,0,1]\n"
    "v_pk_fma_f32 v[126:127], v[78:79], v[144:145], v[126:127] op_sel_hi:[1,0,1]\n"
    "v_pk_mul_f32 v[64:65], v[120:121], v[146:147] op_sel_hi:[1,0]\n"
    "v_pk_mul_f32 v[66:67], v[122:123], v[146:147] op_sel_hi:[1,0]\n"
    "v_pk_mul_f32 v[68:69], v[124:125], v[146:147] op_sel_hi:[1,0]\n"
    "v_pk_mul_f32 v[70:71], v[126:127], v[146:147] op_sel_hi:[1,0]\n"
    "v_exp_f32 v64, v64\n"
    "v_exp_f32 v65, v65\n"
    "v_exp_f32 v66, v66\n"
    "v_exp_f32 v67, v67\n"
    "v_exp_f32 v68, v68\n"
    "v_exp_f32 v69, v69\n"
    "v_exp_f32 v70, v70\n"
    "v_exp_f32 v71, v71\n"
    "v_pk_fma_f32 v[64:65], v[64:65], v[144:145], v[144:145] op_sel:[0,1,1] op_sel_hi:[1,1,1]\n"
    "v_pk_fma_f32 v[66:67], v[66:67], v[144:145], v[144:145] op_sel:[0,1,1] op_sel_hi:[1,1,1]\n"
    "buffer_load_dwordx4 v[160:163], v7, s[12:15], s78 offen sc1\n"
    "v_pk_fma_f32 v[68:69], v[68:69], v[144:145], v[144:145] op_sel:[0,1,1] op_sel_hi:[1,1,1]\n"
    "v_pk_fma_f32 v[70:71], v[70:71], v[144:145], v[144:145] op_sel:[0,1,1] op_sel_hi:[1,1,1]\n"
    "v_rcp_f32 v64, v64\n"
    "v_rcp_f32 v65, v65\n"
    "v_rcp_f32 v66, v66\n"
    "v_rcp_f32 v67, v67\n"
    "v_rcp_f32 v68, v68\n"
    "v_rcp_f32 v69, v69\n"
    "v_rcp_f32 v70, v70\n"
    "v_rcp_f32 v71, v71\n"
    "v_pk_mul_f32 v[64:65], v[64:65], v[120:121]\n"
    "v_pk_mul_f32 v[66:67], v[66:67], v[122:123]\n"
    "v_pk_mul_f32 v[68:69], v[68:69], v[124:125]\n"
    "v_pk_mul_f32 v[70:71], v[70:71], v[126:127]\n"
    "v_cvt_pk_bf16_f32 v120, v64, v65\n"
    "v_cvt_pk_bf16_f32 v121, v66, v67\n"
    "v_cvt_pk_bf16_f32 v122, v68, v69\n"
    "v_cvt_pk_bf16_f32 v123, v70, v71\n"
    "buffer_store_dwordx4 v[120:123], v7, s[12:15], s83 offen sc1\n"
    "v_pk_fma_f32 v[108:109], v[80:81], v[140:141], v[108:109] op_sel_hi:[1,0,1]\n"
    "v_pk_fma_f32 v[110:111], v[82:83], v[140:141], v[110:111] op_sel_hi:[1,0,1]\n"
    "v_pk_fma_f32 v[112:113], v[84:85], v[140:141], v[112:113] op_sel_hi:[1,0,1]\n"
    "v_pk_fma_f32 v[114:115], v[86:87], v[140:141], v[114:115] op_sel_hi:[1,0,1]\n"
    "v_pk_mul_f32 v[64:65], v[108:109], v[142:143] op_sel_hi:[1,0]\n"
    "v_pk_mul_f32 v[66:67], v[110:111], v[142:143] op_sel_hi:[1,0]\n"
    "v_pk_mul_f32 v[68:69], v[112:113], v[142:143] op_sel_hi:[1,0]\n"
    "v_pk_mul_f32 v[70:71], v[114:115], v[142:143] op_sel_hi:[1,0]\n"
    "v_exp_f32 v64, v64\n"
    "buffer_load_dwordx4 v[156:159], v10, s[12:15], s79 offen sc1\n"
    "v_exp_f32 v65, v65\n"
    "v_exp_f32 v66, v66\n"
    "v_exp_f32 v67, v67\n"
    "v_exp_f32 v68, v68\n"
    "v_exp_f32 v69, v69\n"
    "v_exp_f32 v70, v70\n"
    "v_exp_f32 v71, v71\n"
    "v_pk_fma_f32 v[64:65], v[64:65], v[140:141], v[140:141] op_sel:[0,1,1] op_sel_hi:[1,1,1]\n"
    "v_pk_fma_f32 v[66:67], v[66:67], v[140:141], v[140:141] op_sel:[0,1,1] op_sel_hi:[1,1,1]\n"
    "v_pk_fma_f32 v[68:69], v[68:69], v[140:141], v[140:141] op_sel:[0,1,1] op_sel_hi:[1,1,1]\n"
    "v_pk_fma_f32 v[70:71], v[70:71], v[140:141], v[140:141] op_sel:[0,1,1] op_sel_hi:[1,1,1]\n"
    "v_rcp_f32 v64, v64\n"
    "v_rcp_f32 v65, v65\n"
    "v_rcp_f32 v66, v66\n"
    "v_rcp_f32 v67, v67\n"
    "v_rcp_f32 v68, v68\n"
    "v_rcp_f32 v69, v69\n"
    "v_rcp_f32 v70, v70\n"
    "v_rcp_f32 v71, v71\n"
    "v_pk_mul_f32 v[64:65], v[64:65], v[108:109]\n"
    "v_pk_mul_f32 v[66:67], v[66:67], v[110:111]\n"
    "buffer_load_dwordx4 v[152:155], v10, s[12:15], s79 offen offset:1024 sc1\n"
    "v_pk_mul_f32 v[68:69], v[68:69], v[112:113]\n"
    "v_pk_mul_f32 v[70:71], v[70:71], v[114:115]\n"
    "v_cvt_pk_bf16_f32 v108, v64, v65\n"
    "v_cvt_pk_bf16_f32 v109, v66, v67\n"
    "v_cvt_pk_bf16_f32 v110, v68, v69\n"
    "v_cvt_pk_bf16_f32 v111, v70, v71\n"
    "buffer_store_dwordx4 v[108:111], v7, s[12:15], s88 offen offset:1024 sc1\n"
    "v_pk_fma_f32 v[128:129], v[80:81], v[144:145], v[128:129] op_sel_hi:[1,0,1]\n"
    "v_pk_fma_f32 v[130:131], v[82:83], v[144:145], v[130:131] op_sel_hi:[1,0,1]\n"
    "v_pk_fma_f32 v[132:133], v[84:85], v[144:145], v[132:133] op_sel_hi:[1,0,1]\n"
    "v_pk_fma_f32 v[134:135], v[86:87], v[144:145], v[134:135] op_sel_hi:[1,0,1]\n"
    "v_pk_mul_f32 v[64:65], v[128:129], v[146:147] op_sel_hi:[1,0]\n"
    "v_pk_mul_f32 v[66:67], v[130:131], v[146:147] op_sel_hi:[1,0]\n"
    "v_pk_mul_f32 v[68:69], v[132:133], v[146:147] op_sel_hi:[1,0]\n"
    "v_pk_mul_f32 v[70:71], v[134:135], v[146:147] op_sel_hi:[1,0]\n"
    "v_exp_f32 v64, v64\n"
    "v_exp_f32 v65, v65\n"
    "v_exp_f32 v66, v66\n"
    "v_exp_f32 v67, v67\n"
    "v_exp_f32 v68, v68\n"
    "v_exp_f32 v69, v69\n"
    "v_exp_f32 v70, v70\n"
    "v_exp_f32 v71, v71\n"
    "v_pk_fma_f32 v[64:65], v[64:65], v[144:145], v[144:145] op_sel:[0,1,1] op_sel_hi:[1,1,1]\n"
    "v_pk_fma_f32 v[66:67], v[66:67], v[144:145], v[144:145] op_sel:[0,1,1] op_sel_hi:[1,1,1]\n"
    "v_pk_fma_f32 v[68:69], v[68:69], v[144:145], v[144:145] op_sel:[0,1,1] op_sel_hi:[1,1,1]\n"
    "v_pk_fma_f32 v[70:71], v[70:71], v[144:145], v[144:145] op_sel:[0,1,1] op_sel_hi:[1,1,1]\n"
    "v_rcp_f32 v64, v64\n"
    "v_rcp_f32 v65, v65\n"
    "buffer_load_dwordx4 v[148:151], v10, s[12:15], s79 offen offset:2048 sc1\n"
    "v_rcp_f32 v66, v66\n"
    "v_rcp_f32 v67, v67\n"
    "v_rcp_f32 v68, v68\n"
    "v_rcp_f32 v69, v69\n"
    "v_rcp_f32 v70, v70\n"
    "v_rcp_f32 v71, v71\n"
    "v_pk_mul_f32 v[64:65], v[64:65], v[128:129]\n"
    "v_pk_mul_f32 v[66:67], v[66:67], v[130:131]\n"
    "v_pk_mul_f32 v[68:69], v[68:69], v[132:133]\n"
    "v_pk_mul_f32 v[70:71], v[70:71], v[134:135]\n"
    "v_cvt_pk_bf16_f32 v128, v64, v65\n"
    "v_cvt_pk_bf16_f32 v129, v66, v67\n"
    "v_cvt_pk_bf16_f32 v130, v68, v69\n"
    "v_cvt_pk_bf16_f32 v131, v70, v71\n"
    "buffer_store_dwordx4 v[128:131], v7, s[12:15], s83 offen offset:1024 sc1\n"
    "s_waitcnt vmcnt(12)\n"
    "s_barrier\n"
    "ds_read_b128 v[32:35], v26\n"
    "ds_read_b128 v[36:39], v26 offset:2048\n"
    "ds_read_b128 v[40:43], v26 offset:1024\n"
    "ds_read_b128 v[44:47], v26 offset:3072\n"
    "v_pk_fma_f32 v[116:117], v[88:89], v[140:141], v[116:117] op_sel_hi:[1,0,1]\n"
    "v_pk_fma_f32 v[118:119], v[90:91], v[140:141], v[118:119] op_sel_hi:[1,0,1]\n"
    "v_pk_fma_f32 v[136:137], v[88:89], v[144:145], v[136:137] op_sel_hi:[1,0,1]\n"
    "v_pk_fma_f32 v[138:139], v[90:91], v[144:145], v[138:139] op_sel_hi:[1,0,1]\n"
    "v_pk_mul_f32 v[64:65], v[116:117], v[142:143] op_sel_hi:[1,0]\n"
    "v_pk_mul_f32 v[66:67], v[118:119], v[142:143] op_sel_hi:[1,0]\n"
    "v_pk_mul_f32 v[68:69], v[136:137], v[146:147] op_sel_hi:[1,0]\n"
    "v_pk_mul_f32 v[70:71], v[138:139], v[146:147] op_sel_hi:[1,0]\n"
    "v_exp_f32 v64, v64\n"
    "v_exp_f32 v65, v65\n"
    "v_exp_f32 v66, v66\n"
    "v_exp_f32 v67, v67\n"
    "buffer_load_dwordx4 v[76:79], v10, s[12:15], s79 offen offset:3072 sc1\n"
    "v_exp_f32 v68, v68\n"
    "v_exp_f32 v69, v69\n"
    "v_exp_f32 v70, v70\n"
    "v_exp_f32 v71, v71\n"
    "v_pk_fma_f32 v[64:65], v[64:65], v[140:141], v[140:141] op_sel:[0,1,1] op_sel_hi:[1,1,1]\n"
    "v_pk_fma_f32 v[66:67], v[66:67], v[140:141], v[140:141] op_sel:[0,1,1] op_sel_hi:[1,1,1]\n"
    "v_pk_fma_f32 v[68:69], v[68:69], v[144:145], v[144:145] op_sel:[0,1,1] op_sel_hi:[1,1,1]\n"
    "v_pk_fma_f32 v[70:71], v[70:71], v[144:145], v[144:145] op_sel:[0,1,1] op_sel_hi:[1,1,1]\n"
    "v_rcp_f32 v64, v64\n"
    "v_rcp_f32 v65, v65\n"
    "v_rcp_f32 v66, v66\n"
    "v_rcp_f32 v67, v67\n"
    "v_rcp_f32 v68, v68\n"
    "v_rcp_f32 v69, v69\n"
    "v_rcp_f32 v70, v70\n"
    "v_rcp_f32 v71, v71\n"
    "v_pk_mul_f32 v[64:65], v[64:65], v[116:117]\n"
    "v_pk_mul_f32 v[66:67], v[66:67], v[118:119]\n"
    "v_pk_mul_f32 v[68:69], v[68:69], v[136:137]\n"
    "v_pk_mul_f32 v[70:71], v[70:71], v[138:139]\n"
    "v_cvt_pk_bf16_f32 v116, v64, v65\n"
    "v_cvt_pk_bf16_f32 v117, v66, v67\n"
    "v_cvt_pk_bf16_f32 v118, v68, v69\n"
    "s_add_u32 s79, s79, 0x1000\n"
    "buffer_load_dwordx4 v[72:75], v10, s[12:15], s79 offen sc1\n"
    "v_cvt_pk_bf16_f32 v119, v70, v71\n"
    "v_permlane16_swap_b32 v116, v118\n"
    "s_nop 0\n"
    "v_permlane16_swap_b32 v117, v119\n"
    "buffer_store_dwordx4 v[116:119], v27, s[12:15], s82 offen sc1\n"
    "s_waitcnt lgkmcnt(3)\n"
    "v_mfma_f32_16x16x32_bf16 v[100:103], v[32:35], a[0:3], 0\n"
    "ds_read_b128 v[48:51], v26 offset:4096\n"
    "s_waitcnt lgkmcnt(3)\n"
    "v_lshlrev_b32 v1, 10, v6\n"
    "v_mfma_f32_16x16x32_bf16 v[100:103], v[36:39], a[4:7], v[100:103]\n"
    "buffer_load_dwordx4 v[116:119], v7, s[12:15], s78 offen offset:1024 sc1\n"
    "v_mov_b32 v2, 0x80000000\n"
    "s_mov_b64 s[28:29], 15\n"
    "v_mfma_f32_16x16x32_bf16 v[120:123], v[32:35], a[92:95], 0\n"
    "ds_read_b128 v[52:55], v26 offset:6144\n"
    "v_cndmask_b32 v17, v2, v1, s[28:29]\n"
    "s_lshl2_add_u32 s63, s49, s41\n"
    "v_mfma_f32_16x16x32_bf16 v[120:123], v[36:39], a[96:99], v[120:123]\n"
    "s_waitcnt lgkmcnt(3)\n"
    "s_lshl_b32 s63, s63, 12\n"
    "s_add_u32 s63, s63, 0xc0000\n"
    "v_mfma_f32_16x16x32_bf16 v[104:107], v[40:43], a[0:3], 0\n"
    "ds_read_b128 v[56:59], v26 offset:5120\n"
    "s_waitcnt lgkmcnt(3)\n"
    "s_add_u32 s69, s81, 0xd800\n"
    "v_mfma_f32_16x16x32_bf16 v[104:107], v[44:47], a[4:7], v[104:107]\n"
    "buffer_load_dwordx4 v[88:91], v7, s[12:15], s78 offen offset:2048 sc1\n"
    "s_lshl1_add_u32 s43, s49, s52\n"
    "s_mul_i32 s43, s43, 0x36000\n"
    "v_mfma_f32_16x16x32_bf16 v[124:127], v[40:43], a[92:95], 0\n"
    "ds_read_b128 v[60:63], v26 offset:7168\n"
    "s_add_u32 s43, s43, 0xb3c000\n"
    "s_mul_i32 s84, s41, 0x3400\n"
    "v_mfma_f32_16x16x32_bf16 v[124:127], v[44:47], a[96:99], v[124:127]\n"
    "s_waitcnt lgkmcnt(3)\n"
    "s_add_u32 s84, s84, s43\n"
    "s_lshr_b32 s44, s41, 1\n"
    "v_mfma_f32_16x16x32_bf16 v[100:103], v[48:51], a[8:11], v[100:103]\n"
    "ds_read_b128 v[32:35], v26 offset:8192\n"
    "s_waitcnt lgkmcnt(3)\n"
    "s_add_u32 s85, s84, 0x1000\n"
    "v_mfma_f32_16x16x32_bf16 v[100:103], v[52:55], a[12:15], v[100:103]\n"
    "buffer_load_dwordx4 a[96:99], v7, s[12:15], s78 offen offset:3072 sc1\n"
    "s_add_u32 s78, s78, 0x1000\n"
    "s_add_u32 s86, s84, 0x2000\n"
    "v_mfma_f32_16x16x32_bf16 v[120:123], v[48:51], a[100:103], v[120:123]\n"
    "ds_read_b128 v[36:39], v26 offset:10240\n"
    "s_add_u32 s87, s84, 0x2400\n"
    "s_add_u32 s44, s44, 52\n"
    "v_mfma_f32_16x16x32_bf16 v[120:123], v[52:55], a[104:107], v[120:123]\n"
    "s_waitcnt lgkmcnt(3)\n"
    "s_lshl_b32 s44, s44, 10\n"
    "s_add_u32 s68, s43, s44\n"
    "v_mfma_f32_16x16x32_bf16 v[104:107], v[56:59], a[8:11], v[104:107]\n"
    "ds_read_b128 v[40:43], v26 offset:9216\n"
    "s_waitcnt lgkmcnt(3)\n"
    "s_add_u32 s45, s41, 1\n"
    "v_mfma_f32_16x16x32_bf16 v[104:107], v[60:63], a[12:15], v[104:107]\n"
    "buffer_load_dwordx4 a[92:95], v7, s[12:15], s78 offen sc1\n"
    "s_and_b32 s45, s45, 3\n"
    "s_mul_i32 s44, s45, 0xd800\n"
    "v_mfma_f32_16x16x32_bf16 v[124:127], v[56:59], a[100:103], v[124:127]\n"
    "ds_read_b128 v[44:47], v26 offset:11264\n"
    "v_add_u32 v11, s44, v7\n"
    "v_add_u32 v22, s44, v10\n"
    "v_mfma_f32_16x16x32_bf16 v[124:127], v[60:63], a[104:107], v[124:127]\n"
    "s_waitcnt lgkmcnt(3)\n"
    "s_mul_i32 s44, s45, 0x3c00\n"
    "s_add_u32 s44, s44, 0\n"
    "s_waitcnt vmcnt(12)\n"
    "s_barrier\n"
    "s_cmp_lt_u32 s41, 1\n"
    "s_cbranch_scc0 .Lskip_26_%=\n"
    "buffer_store_dwordx2 v[18:19], v29, s[12:15], s56 offen offset:256 sc1\n"
    ".Lskip_26_%=:\n"
    "s_cmp_eq_u32 s40, 0\n"
    "s_cbranch_scc0 .Lskip_27_%=\n"
    "buffer_store_dwordx2 v[18:19], v29, s[12:15], 0 offen sc1\n"
    ".Lskip_27_%=:\n"
    "v_mfma_f32_16x16x32_bf16 v[100:103], v[32:35], a[28:31], v[100:103]\n"
    "ds_read_b128 v[48:51], v26 offset:12288\n"
    "s_waitcnt lgkmcnt(3)\n"
    "v_add_u32 v30, s44, v7\n"
    "v_mfma_f32_16x16x32_bf16 v[100:103], v[36:39], a[32:35], v[100:103]\n"
    "buffer_load_dwordx4 a[104:107], v7, s[12:15], s78 offen offset:1024 sc1\n"
    "s_add_u32 s45, s41, 2\n"
    "s_and_b32 s45, s45, 3\n"
    "v_mfma_f32_16x16x32_bf16 v[120:123], v[32:35], a[108:111], v[120:123]\n"
    "ds_read_b128 v[52:55], v26 offset:14336\n"
    "s_mul_i32 s44, s45, 0xd800\n"
    "v_add_u32 v16, s44, v7\n"
    "v_mfma_f32_16x16x32_bf16 v[120:123], v[36:39], a[112:115], v[120:123]\n"
    "s_waitcnt lgkmcnt(3)\n"
    "v_add_u32 v23, s44, v10\n"
    "s_mul_i32 s44, s45, 0x3c00\n"
    "v_mfma_f32_16x16x32_bf16 v[104:107], v[40:43], a[28:31], v[104:107]\n"
    "ds_read_b128 v[56:59], v26 offset:13312\n"
    "s_waitcnt lgkmcnt(3)\n"
    "s_add_u32 s44, s44, 0x1400\n"
    "v_mfma_f32_16x16x32_bf16 v[104:107], v[44:47], a[32:35], v[104:107]\n"
    "buffer_load_dwordx4 a[100:103], v7, s[12:15], s78 offen offset:2048 sc1\n"
    "v_add_u32 v31, s44, v7\n"
    "s_add_u32 s45, s41, 3\n"
    "v_mfma_f32_16x16x32_bf16 v[124:127], v[40:43], a[108:111], v[124:127]\n"
    "ds_read_b128 v[60:63], v26 offset:15360\n"
    "s_and_b32 s45, s45, 3\n"
    "s_mul_i32 s44, s45, 0xd800\n"
    "v_mfma_f32_16x16x32_bf16 v[124:127], v[44:47], a[112:115], v[124:127]\n"
    "s_waitcnt lgkmcnt(3)\n"
    "v_add_u32 v20, s44, v7\n"
    "v_add_u32 v24, s44, v10\n"
    "v_mfma_f32_16x16x32_bf16 v[100:103], v[48:51], a[36:39], v[100:103]\n"
    "ds_read_b128 v[32:35], v26 offset:16384\n"
    "s_waitcnt lgkmcnt(3)\n"
    "s_mul_i32 s44, s45, 0x3c00\n"
    "v_mfma_f32_16x16x32_bf16 v[100:103], v[52:55], a[40:43], v[100:103]\n"
    "buffer_load_dwordx4 a[112:115], v7, s[12:15], s78 offen offset:3072 sc1\n"
    "s_add_u32 s78, s78, 0x1000\n"
    "s_add_u32 s44, s44, 0x2800\n"
    "v_mfma_f32_16x16x32_bf16 v[120:123], v[48:51], a[116:119], v[120:123]\n"
    "ds_read_b128 v[36:39], v26 offset:18432\n"
    "v_add_u32 v80, s44, v7\n"
    "s_add_u32 s45, s41, 4\n"
    "v_mfma_f32_16x16x32_bf16 v[120:123], v[52:55], a[120:123], v[120:123]\n"
    "s_waitcnt lgkmcnt(3)\n"
    "s_and_b32 s45, s45, 3\n"
    "s_mul_i32 s44, s45, 0xd800\n"
    "v_mfma_f32_16x16x32_bf16 v[104:107], v[56:59], a[36:39], v[104:107]\n"
    "ds_read_b128 v[40:43], v26 offset:17408\n"
    "s_waitcnt lgkmcnt(3)\n"
    "v_add_u32 v21, s44, v7\n"
    "v_mfma_f32_16x16x32_bf16 v[104:107], v[60:63], a[40:43], v[104:107]\n"
    "buffer_load_dwordx4 a[108:111], v7, s[12:15], s78 offen sc1\n"
    "v_add_u32 v25, s44, v10\n"
    "s_mul_i32 s44, s45, 0x3c00\n"
    "v_mfma_f32_16x16x32_bf16 v[124:127], v[56:59], a[116:119], v[124:127]\n"
    "ds_read_b128 v[44:47], v26 offset:19456\n"
    "s_add_u32 s44, s44, 0\n"
    "v_add_u32 v81, s44, v7\n"
    "v_mfma_f32_16x16x32_bf16 v[124:127], v[60:63], a[120:123], v[124:127]\n"
    "s_waitcnt lgkmcnt(3)\n"
    "v_mul_u32_u24 v28, 0xa00, v8\n"
    "v_lshl_add_u32 v27, v9, 4, v28\n"
    "v_mfma_f32_16x16x32_bf16 v[100:103], v[32:35], a[44:47], v[100:103]\n"
    "ds_read_b128 v[48:51], v26 offset:20480\n"
    "s_waitcnt lgkmcnt(3)\n"
    "v_lshl_add_u32 v28, v9, 3, v28\n"
    "v_mfma_f32_16x16x32_bf16 v[100:103], v[36:39], a[48:51], v[100:103]\n"
    "buffer_load_dwordx4 a[120:123], v7, s[12:15], s78 offen offset:1024 sc1\n"
    "s_lshl_b32 s65, s70, 5\n"
    "s_add_u32 s65, s65, s64\n"
    "v_mfma_f32_16x16x32_bf16 v[120:123], v[32:35], a[124:127], v[120:123]\n"
    "ds_read_b128 v[52:55], v26 offset:22528\n"
    "s_xor_b32 s62, s70, 1\n"
    "s_lshl_b32 s62, s62, 7\n"
    "v_mfma_f32_16x16x32_bf16 v[120:123], v[36:39], a[128:131], v[120:123]\n"
    "s_waitcnt lgkmcnt(3)\n"
    "s_add_u32 s62, s62, s64\n"
    "s_mul_i32 s43, s41, 0xc00\n"
    "v_mfma_f32_16x16x32_bf16 v[104:107], v[40:43], a[44:47], v[104:107]\n"
    "ds_read_b128 v[56:59], v26 offset:21504\n"
    "s_waitcnt lgkmcnt(3)\n"
    "s_add_u32 s43, s43, 0x10000\n"
    "v_mfma_f32_16x16x32_bf16 v[104:107], v[44:47], a[48:51], v[104:107]\n"
    "buffer_load_dwordx4 a[116:119], v7, s[12:15], s78 offen offset:2048 sc1\n"
    "v_lshrrev_b32 v1, 2, v8\n"
    "v_xor_b32 v1, v1, v9\n"
    "v_mfma_f32_16x16x32_bf16 v[124:127], v[40:43], a[124:127], v[124:127]\n"
    "ds_read_b128 v[60:63], v26 offset:23552\n"
    "v_lshl_add_u32 v1, v8, 2, v1\n"
    "v_lshl_add_u32 v82, v1, 4, s43\n"
    "v_mfma_f32_16x16x32_bf16 v[124:127], v[44:47], a[128:131], v[124:127]\n"
    "s_waitcnt lgkmcnt(3)\n"
    "v_lshl_add_u32 v1, v8, 2, v9\n"
    "v_lshl_add_u32 v83, v1, 3, s43\n"
    "v_mfma_f32_16x16x32_bf16 v[100:103], v[48:51], a[52:55], v[100:103]\n"
    "ds_read_b128 v[32:35], v26 offset:24576\n"
    "s_waitcnt lgkmcnt(3)\n"
    "v_xor_b32_e64 v1, v6, v9\n"
    "v_mfma_f32_16x16x32_bf16 v[100:103], v[52:55], a[56:59], v[100:103]\n"
    "buffer_load_dwordx4 a[128:131], v7, s[12:15], s78 offen offset:3072 sc1\n"
    "s_add_u32 s78, s78, 0x1000\n"
    "v_lshl_add_u32 v84, v1, 4, s43\n"
    "v_mfma_f32_16x16x32_bf16 v[120:123], v[48:51], a[132:135], v[120:123]\n"
    "ds_read_b128 v[36:39], v26 offset:26624\n"
    "v_add_u32 v85, s43, v7\n"
    "v_lshrrev_b32 v1, 2, v6\n"
    "v_mfma_f32_16x16x32_bf16 v[120:123], v[52:55], a[136:139], v[120:123]\n"
    "buffer_load_dwordx2 v[12:13], v17, s[12:15], s63 offen offset:256 sc1\n"
    "s_waitcnt lgkmcnt(3)\n"
    "v_mul_u32_u24 v1, 0xa00, v1\n"
    "v_mfma_f32_16x16x32_bf16 v[104:107], v[56:59], a[52:55], v[104:107]\n"
    "ds_read_b128 v[40:43], v26 offset:25600\n"
    "s_waitcnt lgkmcnt(3)\n"
    "v_and_b32 v2, 3, v6\n"
    "v_mfma_f32_16x16x32_bf16 v[104:107], v[60:63], a[56:59], v[104:107]\n"
    "buffer_load_dwordx4 a[124:127], v7, s[12:15], s78 offen sc1\n"
    "v_lshl_add_u32 v27, v2, 4, v1\n"
    "v_lshrrev_b32 v1, 1, v6\n"
    "v_mfma_f32_16x16x32_bf16 v[124:127], v[56:59], a[132:135], v[124:127]\n"
    "ds_read_b128 v[44:47], v26 offset:27648\n"
    "v_mul_u32_u24 v1, 0xa00, v1\n"
    "v_and_b32 v2, 1, v6\n"
    "v_mfma_f32_16x16x32_bf16 v[124:127], v[60:63], a[136:139], v[124:127]\n"
    "s_waitcnt lgkmcnt(3)\n"
    "v_lshl_add_u32 v1, v2, 4, v1\n"
    "v_lshrrev_b32 v2, 5, v6\n"
    "v_mfma_f32_16x16x32_bf16 v[100:103], v[32:35], a[60:63], v[100:103]\n"
    "ds_read_b128 v[48:51], v26 offset:28672\n"
    "s_waitcnt lgkmcnt(3)\n"
    "v_lshl_or_b32 v28, v2, 31, v1\n"
    "v_mfma_f32_16x16x32_bf16 v[100:103], v[36:39], a[64:67], v[100:103]\n"
    "buffer_load_dwordx4 a[136:139], v7, s[12:15], s78 offen offset:1024 sc1\n"
    "v_mfma_f32_16x16x32_bf16 v[120:123], v[32:35], a[140:143], v[120:123]\n"
    "ds_read_b128 v[52:55], v26 offset:30720\n"
    "v_mfma_f32_16x16x32_bf16 v[120:123], v[36:39], a[144:147], v[120:123]\n"
    "s_waitcnt lgkmcnt(3)\n"
    "v_mfma_f32_16x16x32_bf16 v[104:107], v[40:43], a[60:63], v[104:107]\n"
    "ds_read_b128 v[56:59], v26 offset:29696\n"
    "s_waitcnt lgkmcnt(3)\n"
    "v_mfma_f32_16x16x32_bf16 v[104:107], v[44:47], a[64:67], v[104:107]\n"
    "buffer_load_dwordx4 a[132:135], v7, s[12:15], s78 offen offset:2048 sc1\n"
    "v_mfma_f32_16x16x32_bf16 v[124:127], v[40:43], a[140:143], v[124:127]\n"
    "ds_read_b128 v[60:63], v26 offset:31744\n"
    "v_mfma_f32_16x16x32_bf16 v[124:127], v[44:47], a[144:147], v[124:127]\n"
    "s_waitcnt lgkmcnt(3)\n"
    "v_mfma_f32_16x16x32_bf16 v[100:103], v[48:51], a[68:71], v[100:103]\n"
    "ds_read_b128 v[32:35], v26 offset:32768\n"
    "s_waitcnt lgkmcnt(3)\n"
    "v_mfma_f32_16x16x32_bf16 v[100:103], v[52:55], a[72:75], v[100:103]\n"
    "buffer_load_dwordx4 a[144:147], v7, s[12:15], s78 offen offset:3072 sc1\n"
    "s_add_u32 s78, s78, 0x1000\n"
    "v_mfma_f32_16x16x32_bf16 v[120:123], v[48:51], a[148:151], v[120:123]\n"
    "ds_read_b128 v[36:39], v26 offset:34816\n"
    "v_mfma_f32_16x16x32_bf16 v[120:123], v[52:55], a[152:155], v[120:123]\n"
    "s_waitcnt lgkmcnt(3)\n"
    "v_mfma_f32_16x16x32_bf16 v[104:107], v[56:59], a[68:71], v[104:107]\n"
    "ds_read_b128 v[40:43], v26 offset:33792\n"
    "s_waitcnt lgkmcnt(3)\n"
    "v_mfma_f32_16x16x32_bf16 v[104:107], v[60:63], a[72:75], v[104:107]\n"
    "buffer_load_dwordx4 a[140:143], v7, s[12:15], s78 offen sc1\n"
    "v_mfma_f32_16x16x32_bf16 v[124:127], v[56:59], a[148:151], v[124:127]\n"
    "ds_read_b128 v[44:47], v26 offset:35840\n"
    "v_mfma_f32_16x16x32_bf16 v[124:127], v[60:63], a[152:155], v[124:127]\n"
    "s_waitcnt lgkmcnt(3)\n"
    "v_mfma_f32_16x16x32_bf16 v[100:103], v[32:35], a[76:79], v[100:103]\n"
    "ds_read_b128 v[48:51], v26 offset:36864\n"
    "s_waitcnt lgkmcnt(3)\n"
    "v_mfma_f32_16x16x32_bf16 v[100:103], v[36:39], a[80:83], v[100:103]\n"
    "buffer_load_dwordx4 a[152:155], v7, s[12:15], s78 offen offset:1024 sc1\n"
    "v_mfma_f32_16x16x32_bf16 v[120:123], v[32:35], a[156:159], v[120:123]\n"
    "ds_read_b128 v[52:55], v26 offset:38912\n"
    "v_mfma_f32_16x16x32_bf16 v[120:123], v[36:39], a[160:163], v[120:123]\n"
    "s_waitcnt lgkmcnt(3)\n"
    "v_mfma_f32_16x16x32_bf16 v[104:107], v[40:43], a[76:79], v[104:107]\n"
    "ds_read_b128 v[56:59], v26 offset:37888\n"
    "s_waitcnt lgkmcnt(3)\n"
    "v_mfma_f32_16x16x32_bf16 v[104:107], v[44:47], a[80:83], v[104:107]\n"
    "buffer_load_dwordx4 a[148:151], v7, s[12:15], s78 offen offset:2048 sc1\n"
    "v_mfma_f32_16x16x32_bf16 v[124:127], v[40:43], a[156:159], v[124:127]\n"
    "ds_read_b128 v[60:63], v26 offset:39936\n"
    "v_mfma_f32_16x16x32_bf16 v[124:127], v[44:47], a[160:163], v[124:127]\n"
    "s_waitcnt lgkmcnt(3)\n"
    "s_waitcnt vmcnt(19)\n"
    "s_barrier\n"
    "s_cmp_lt_u32 s41, 1\n"
    "s_cbranch_scc0 .Lskip_28_%=\n"
    "buffer_store_dwordx2 v[18:19], v29, s[12:15], s56 offen offset:384 sc1\n"
    ".Lskip_28_%=:\n"
    "v_mfma_f32_16x16x32_bf16 v[100:103], v[48:51], a[84:87], v[100:103]\n"
    "s_waitcnt lgkmcnt(2)\n"
    "v_mfma_f32_16x16x32_bf16 v[100:103], v[52:55], a[88:91], v[100:103]\n"
    "buffer_load_dwordx4 a[160:163], v7, s[12:15], s78 offen offset:3072 sc1\n"
    "s_add_u32 s78, s78, 0x1000\n"
    "v_mfma_f32_16x16x32_bf16 v[120:123], v[48:51], a[164:167], v[120:123]\n"
    "v_mfma_f32_16x16x32_bf16 v[120:123], v[52:55], a[168:171], v[120:123]\n"
    "s_waitcnt lgkmcnt(1)\n"
    "v_mfma_f32_16x16x32_bf16 v[104:107], v[56:59], a[84:87], v[104:107]\n"
    "s_waitcnt lgkmcnt(0)\n"
    "v_mfma_f32_16x16x32_bf16 v[104:107], v[60:63], a[88:91], v[104:107]\n"
    "buffer_load_dwordx4 a[156:159], v7, s[12:15], s78 offen sc1\n"
    "v_mfma_f32_16x16x32_bf16 v[124:127], v[56:59], a[164:167], v[124:127]\n"
    "v_mfma_f32_16x16x32_bf16 v[124:127], v[60:63], a[168:171], v[124:127]\n"
    "s_waitcnt vmcnt(9)\n"
    "v_cmp_ne_u64_e64 vcc, s[38:39], v[12:13]\n"
    "s_nop 0\n"
    "s_and_b64 vcc, vcc, s[28:29]\n"
    "s_cbranch_scc0 .Lok_30_%=\n"
    "s_movk_i32 s46, 0x4e20\n"
    ".Lspin_29_%=:\n"
    "buffer_load_dwordx2 v[12:13], v17, s[12:15], s63 offen offset:256 sc1\n"
    "s_sub_u32 s46, s46, 1\n"
    "s_cbranch_scc1 .Lok_30_%=\n"
    "s_waitcnt vmcnt(0)\n"
    "v_cmp_ne_u64 vcc, s[38:39], v[12:13]\n"
    "s_nop 0\n"
    "s_and_b64 vcc, vcc, s[28:29]\n"
    "s_cbranch_scc1 .Lspin_29_%=\n"
    ".Lok_30_%=:\n"
    "v_pk_fma_f32 v[100:101], v[92:93], v[140:141], v[100:101] op_sel_hi:[1,0,1]\n"
    "buffer_load_dwordx4 a[72:75], v11, s[12:15], s84 offen sc1\n"
    "v_pk_fma_f32 v[102:103], v[94:95], v[140:141], v[102:103] op_sel_hi:[1,0,1]\n"
    "v_pk_fma_f32 v[104:105], v[96:97], v[140:141], v[104:105] op_sel_hi:[1,0,1]\n"
    "v_pk_fma_f32 v[106:107], v[98:99], v[140:141], v[106:107] op_sel_hi:[1,0,1]\n"
    "v_pk_mul_f32 v[64:65], v[100:101], v[142:143] op_sel_hi:[1,0]\n"
    "v_pk_mul_f32 v[66:67], v[102:103], v[142:143] op_sel_hi:[1,0]\n"
    "v_pk_mul_f32 v[68:69], v[104:105], v[142:143] op_sel_hi:[1,0]\n"
    "v_pk_mul_f32 v[70:71], v[106:107], v[142:143] op_sel_hi:[1,0]\n"
    "v_exp_f32 v64, v64\n"
    "v_exp_f32 v65, v65\n"
    "v_exp_f32 v66, v66\n"
    "v_exp_f32 v67, v67\n"
    "v_exp_f32 v68, v68\n"
    "buffer_load_dwordx4 a[76:79], v11, s[12:15], s84 offen offset:2048 sc1\n"
    "v_exp_f32 v69, v69\n"
    "v_exp_f32 v70, v70\n"
    "v_exp_f32 v71, v71\n"
    "v_pk_fma_f32 v[64:65], v[64:65], v[140:141], v[140:141] op_sel:[0,1,1] op_sel_hi:[1,1,1]\n"
    "v_pk_fma_f32 v[66:67], v[66:67], v[140:141], v[140:141] op_sel:[0,1,1] op_sel_hi:[1,1,1]\n"
    "v_pk_fma_f32 v[68:69], v[68:69], v[140:141], v[140:141] op_sel:[0,1,1] op_sel_hi:[1,1,1]\n"
    "v_pk_fma_f32 v[70:71], v[70:71], v[140:141], v[140:141] op_sel:[0,1,1] op_sel_hi:[1,1,1]\n"
    "v_rcp_f32 v64, v64\n"
    "v_rcp_f32 v65, v65\n"
    "v_rcp_f32 v66, v66\n"
    "v_rcp_f32 v67, v67\n"
    "buffer_load_dwordx4 a[80:83], v16, s[12:15], s84 offen sc1\n"
    "v_rcp_f32 v68, v68\n"
    "v_rcp_f32 v69, v69\n"
    "v_rcp_f32 v70, v70\n"
    "v_rcp_f32 v71, v71\n"
    "v_pk_mul_f32 v[64:65], v[64:65], v[100:101]\n"
    "v_pk_mul_f32 v[66:67], v[66:67], v[102:103]\n"
    "v_pk_mul_f32 v[68:69], v[68:69], v[104:105]\n"
    "v_pk_mul_f32 v[70:71], v[70:71], v[106:107]\n"
    "v_cvt_pk_bf16_f32 v100, v64, v65\n"
    "v_cvt_pk_bf16_f32 v101, v66, v67\n"
    "v_cvt_pk_bf16_f32 v102, v68, v69\n"
    "v_cvt_pk_bf16_f32 v103, v70, v71\n"
    "v_pk_fma_f32 v[120:121], v[92:93], v[144:145], v[120:121] op_sel_hi:[1,0,1]\n"
    "buffer_load_dwordx4 a[84:87], v16, s[12:15], s84 offen offset:2048 sc1\n"
    "v_pk_fma_f32 v[122:123], v[94:95], v[144:145], v[122:123] op_sel_hi:[1,0,1]\n"
    "v_pk_fma_f32 v[124:125], v[96:97], v[144:145], v[124:125] op_sel_hi:[1,0,1]\n"
    "v_pk_fma_f32 v[126:127], v[98:99], v[144:145], v[126:127] op_sel_hi:[1,0,1]\n"
    "v_pk_mul_f32 v[64:65], v[120:121], v[146:147] op_sel_hi:[1,0]\n"
    "v_pk_mul_f32 v[66:67], v[122:123], v[146:147] op_sel_hi:[1,0]\n"
    "v_pk_mul_f32 v[68:69], v[124:125], v[146:147] op_sel_hi:[1,0]\n"
    "v_pk_mul_f32 v[70:71], v[126:127], v[146:147] op_sel_hi:[1,0]\n"
    "v_exp_f32 v64, v64\n"
    "v_exp_f32 v65, v65\n"
    "v_exp_f32 v66, v66\n"
    "v_exp_f32 v67, v67\n"
    "v_exp_f32 v68, v68\n"
    "buffer_load_dwordx4 a[88:91], v20, s[12:15], s84 offen sc1\n"
    "v_exp_f32 v69, v69\n"
    "v_exp_f32 v70, v70\n"
    "v_exp_f32 v71, v71\n"
    "v_pk_fma_f32 v[64:65], v[64:65], v[144:145], v[144:145] op_sel:[0,1,1] op_sel_hi:[1,1,1]\n"
    "v_pk_fma_f32 v[66:67], v[66:67], v[144:145], v[144:145] op_sel:[0,1,1] op_sel_hi:[1,1,1]\n"
    "v_pk_fma_f32 v[68:69], v[68:69], v[144:145], v[144:145] op_sel:[0,1,1] op_sel_hi:[1,1,1]\n"
    "v_pk_fma_f32 v[70:71], v[70:71], v[144:145], v[144:145] op_sel:[0,1,1] op_sel_hi:[1,1,1]\n"
    "v_rcp_f32 v64, v64\n"
    "v_rcp_f32 v65, v65\n"
    "v_rcp_f32 v66, v66\n"
    "v_rcp_f32 v67, v67\n"
    "buffer_load_dwordx4 a[164:167], v20, s[12:15], s84 offen offset:2048 sc1\n"
    "v_rcp_f32 v68, v68\n"
    "v_rcp_f32 v69, v69\n"
    "v_rcp_f32 v70, v70\n"
    "v_rcp_f32 v71, v71\n"
    "v_pk_mul_f32 v[64:65], v[64:65], v[120:121]\n"
    "v_pk_mul_f32 v[66:67], v[66:67], v[122:123]\n"
    "v_pk_mul_f32 v[68:69], v[68:69], v[124:125]\n"
    "v_pk_mul_f32 v[70:71], v[70:71], v[126:127]\n"
    "v_cvt_pk_bf16_f32 v120, v64, v65\n"
    "v_cvt_pk_bf16_f32 v121, v66, v67\n"
    "v_cvt_pk_bf16_f32 v122, v68, v69\n"
    "v_cvt_pk_bf16_f32 v123, v70, v71\n"
    "buffer_load_dwordx2 v[12:13], v17, s[12:15], s63 offen offset:384 sc1\n"
    "buffer_load_dwordx4 a[168:171], v21, s[12:15], s84 offen sc1\n"
    "buffer_store_dwordx4 v[100:103], v7, s[12:15], s81 offen sc1\n"
    "buffer_store_dwordx4 v[120:123], v7, s[12:15], s69 offen sc1\n"
    "v_lshlrev_b32 v108, 16, v212\n"
    "v_and_b32 v109, s58, v212\n"
    "v_lshlrev_b32 v110, 16, v213\n"
    "v_and_b32 v111, s58, v213\n"
    "v_lshlrev_b32 v112, 16, v214\n"
    "v_and_b32 v113, s58, v214\n"
    "v_lshlrev_b32 v114, 16, v215\n"
    "v_and_b32 v115, s58, v215\n"
    "v_lshlrev_b32 v128, 16, v216\n"
    "v_and_b32 v129, s58, v216\n"
    "v_lshlrev_b32 v130, 16, v217\n"
    "v_and_b32 v131, s58, v217\n"
    "v_lshlrev_b32 v132, 16, v218\n"
    "v_and_b32 v133, s58, v218\n"
    "v_lshlrev_b32 v134, 16, v219\n"
    "v_and_b32 v135, s58, v219\n"
    "v_lshlrev_b32 v136, 16, v220\n"
    "v_and_b32 v137, s58, v220\n"
    "buffer_load_dwordx4 v[32:35], v21, s[12:15], s84 offen offset:2048 sc1\n"
    "v_lshlrev_b32 v138, 16, v221\n"
    "v_and_b32 v139, s58, v221\n"
    "s_nop 12\n"
    "buffer_load_dwordx4 v[36:39], v11, s[12:15], s85 offen sc1\n"
    "s_nop 14\n"
    "buffer_load_dwordx4 v[40:43], v11, s[12:15], s85 offen offset:2048 sc1\n"
    "s_nop 14\n"
    "buffer_load_dwordx4 v[44:47], v16, s[12:15], s85 offen sc1\n"
    "s_waitcnt vmcnt(13)\n"
    "v_mfma_f32_16x16x32_bf16 a[0:3], a[252:255], a[72:75], 0\n"
    "s_waitcnt vmcnt(12)\n"
    "v_mfma_f32_16x16x32_bf16 a[0:3], a[212:215], a[76:79], a[0:3]\n"
    "buffer_load_dwordx4 v[48:51], v16, s[12:15], s85 offen offset:2048 sc1\n"
    "v_mfma_f32_16x16x32_bf16 a[4:7], a[248:251], a[72:75], 0\n"
    "v_mfma_f32_16x16x32_bf16 a[4:7], a[208:211], a[76:79], a[4:7]\n"
    "v_mfma_f32_16x16x32_bf16 a[8:11], a[244:247], a[72:75], 0\n"
    "v_mfma_f32_16x16x32_bf16 a[8:11], a[24:27], a[76:79], a[8:11]\n"
    "v_mfma_f32_16x16x32_bf16 a[12:15], a[240:243], a[72:75], 0\n"
    "buffer_load_dwordx4 v[52:55], v20, s[12:15], s85 offen sc1\n"
    "v_mfma_f32_16x16x32_bf16 a[12:15], a[20:23], a[76:79], a[12:15]\n"
    "v_mfma_f32_16x16x32_bf16 a[28:31], a[236:239], a[72:75], 0\n"
    "v_mfma_f32_16x16x32_bf16 a[28:31], a[16:19], a[76:79], a[28:31]\n"
    "s_waitcnt vmcnt(13)\n"
    "v_mfma_f32_16x16x32_bf16 a[32:35], a[252:255], a[80:83], 0\n"
    "s_waitcnt vmcnt(12)\n"
    "v_mfma_f32_16x16x32_bf16 a[32:35], a[212:215], a[84:87], a[32:35]\n"
    "buffer_load_dwordx4 a[72:75], v20, s[12:15], s85 offen offset:2048 sc1\n"
    "v_mfma_f32_16x16x32_bf16 a[36:39], a[248:251], a[80:83], 0\n"
    "v_mfma_f32_16x16x32_bf16 a[36:39], a[208:211], a[84:87], a[36:39]\n"
    "v_mfma_f32_16x16x32_bf16 a[40:43], a[244:247], a[80:83], 0\n"
    "v_mfma_f32_16x16x32_bf16 a[40:43], a[24:27], a[84:87], a[40:43]\n"
    "v_mfma_f32_16x16x32_bf16 a[44:47], a[240:243], a[80:83], 0\n"
    "buffer_load_dwordx4 a[76:79], v21, s[12:15], s85 offen sc1\n"
    "v_mfma_f32_16x16x32_bf16 a[44:47], a[20:23], a[84:87], a[44:47]\n"
    "v_mfma_f32_16x16x32_bf16 a[48:51], a[236:239], a[80:83], 0\n"
    "v_mfma_f32_16x16x32_bf16 a[48:51], a[16:19], a[84:87], a[48:51]\n"
    "s_waitcnt vmcnt(13)\n"
    "v_mfma_f32_16x16x32_bf16 a[52:55], a[252:255], a[88:91], 0\n"
    "s_waitcnt vmcnt(12)\n"
    "v_mfma_f32_16x16x32_bf16 a[52:55], a[212:215], a[164:167], a[52:55]\n"
    "buffer_load_dwordx4 a[80:83], v21, s[12:15], s85 offen offset:2048 sc1\n"
    "v_mfma_f32_16x16x32_bf16 a[56:59], a[248:251], a[88:91], 0\n"
    "v_mfma_f32_16x16x32_bf16 a[56:59], a[208:211], a[164:167], a[56:59]\n"
    "v_mfma_f32_16x16x32_bf16 a[60:63], a[244:247], a[88:91], 0\n"
    "v_mfma_f32_16x16x32_bf16 a[60:63], a[24:27], a[164:167], a[60:63]\n"
    "v_mfma_f32_16x16x32_bf16 a[64:67], a[240:243], a[88:91], 0\n"
    "s_waitcnt vmcnt(12)\n"
    "v_cmp_ne_u64 vcc, s[38:39], v[12:13]\n"
    "s_nop 0\n"
    "s_and_b64 vcc, vcc, s[28:29]\n"
    "s_cbranch_scc0 .Lok_32_%=\n"
    "s_movk_i32 s46, 0x4e20\n"
    ".Lspin_31_%=:\n"
    "buffer_load_dwordx2 v[12:13], v17, s[12:15], s63 offen offset:384 sc1\n"
    "s_sub_u32 s46, s46, 1\n"
    "s_cbranch_scc1 .Lok_32_%=\n"
    "s_waitcnt vmcnt(0)\n"
    "v_cmp_ne_u64 vcc, s[38:39], v[12:13]\n"
    "s_nop 0\n"
    "s_and_b64 vcc, vcc, s[28:29]\n"
    "s_cbranch_scc1 .Lspin_31_%=\n"
    ".Lok_32_%=:\n"
    "buffer_load_dwordx4 a[84:87], v11, s[12:15], s84 offen offset:1024 sc1\n"
    "v_mfma_f32_16x16x32_bf16 a[64:67], a[20:23], a[164:167], a[64:67]\n"
    "v_mfma_f32_16x16x32_bf16 a[68:71], a[236:239], a[88:91], 0\n"
    "v_mfma_f32_16x16x32_bf16 a[68:71], a[16:19], a[164:167], a[68:71]\n"
    "s_waitcnt vmcnt(12)\n"
    "v_mfma_f32_16x16x32_bf16 v[108:111], a[252:255], a[168:171], v[108:111]\n"
    "s_waitcnt vmcnt(9)\n"
    "v_mfma_f32_16x16x32_bf16 v[108:111], a[212:215], v[32:35], v[108:111]\n"
    "buffer_load_dwordx4 a[88:91], v11, s[12:15], s84 offen offset:3072 sc1\n"
    "v_mfma_f32_16x16x32_bf16 v[112:115], a[248:251], a[168:171], v[112:115]\n"
    "v_mfma_f32_16x16x32_bf16 v[112:115], a[208:211], v[32:35], v[112:115]\n"
    "v_mfma_f32_16x16x32_bf16 v[128:131], a[244:247], a[168:171], v[128:131]\n"
    "v_mfma_f32_16x16x32_bf16 v[128:131], a[24:27], v[32:35], v[128:131]\n"
    "v_mfma_f32_16x16x32_bf16 v[132:135], a[240:243], a[168:171], v[132:135]\n"
    "buffer_load_dwordx4 a[164:167], v16, s[12:15], s84 offen offset:1024 sc1\n"
    "v_mfma_f32_16x16x32_bf16 v[132:135], a[20:23], v[32:35], v[132:135]\n"
    "v_mfma_f32_16x16x32_bf16 v[136:139], a[236:239], a[168:171], v[136:139]\n"
    "v_mfma_f32_16x16x32_bf16 v[136:139], a[16:19], v[32:35], v[136:139]\n"
    "s_barrier\n"
    "s_cmp_lt_u32 s41, 1\n"
    "s_cbranch_scc0 .Lskip_33_%=\n"
    "buffer_store_dwordx2 v[18:19], v29, s[12:15], s56 offen offset:512 sc1\n"
    ".Lskip_33_%=:\n"
    "s_waitcnt vmcnt(10)\n"
    "v_mfma_f32_16x16x32_bf16 a[0:3], a[184:187], v[36:39], a[0:3]\n"
    "s_waitcnt vmcnt(9)\n"
    "v_mfma_f32_16x16x32_bf16 a[0:3], v[228:231], v[40:43], a[0:3]\n"
    "buffer_load_dwordx4 a[16:19], v16, s[12:15], s84 offen offset:3072 sc1\n"
    "v_mfma_f32_16x16x32_bf16 a[4:7], a[180:183], v[36:39], a[4:7]\n"
    "v_mfma_f32_16x16x32_bf16 a[4:7], v[224:227], v[40:43], a[4:7]\n"
    "v_mfma_f32_16x16x32_bf16 a[8:11], a[176:179], v[36:39], a[8:11]\n"
    "v_mfma_f32_16x16x32_bf16 a[8:11], v[208:211], v[40:43], a[8:11]\n"
    "v_mfma_f32_16x16x32_bf16 a[12:15], a[172:175], v[36:39], a[12:15]\n"
    "buffer_load_dwordx4 a[20:23], v20, s[12:15], s84 offen offset:1024 sc1\n"
    "v_mfma_f32_16x16x32_bf16 a[12:15], v[204:207], v[40:43], a[12:15]\n"
    "v_mfma_f32_16x16x32_bf16 a[28:31], v[252:255], v[36:39], a[28:31]\n"
    "v_mfma_f32_16x16x32_bf16 a[28:31], v[200:203], v[40:43], a[28:31]\n"
    "s_waitcnt vmcnt(10)\n"
    "v_mfma_f32_16x16x32_bf16 a[32:35], a[184:187], v[44:47], a[32:35]\n"
    "s_waitcnt vmcnt(9)\n"
    "v_mfma_f32_16x16x32_bf16 a[32:35], v[228:231], v[48:51], a[32:35]\n"
    "buffer_load_dwordx4 a[24:27], v20, s[12:15], s84 offen offset:3072 sc1\n"
    "v_mfma_f32_16x16x32_bf16 a[36:39], a[180:183], v[44:47], a[36:39]\n"
    "v_mfma_f32_16x16x32_bf16 a[36:39], v[224:227], v[48:51], a[36:39]\n"
    "v_mfma_f32_16x16x32_bf16 a[40:43], a[176:179], v[44:47], a[40:43]\n"
    "v_mfma_f32_16x16x32_bf16 a[40:43], v[208:211], v[48:51], a[40:43]\n"
    "v_mfma_f32_16x16x32_bf16 a[44:47], a[172:175], v[44:47], a[44:47]\n"
    "buffer_load_dwordx4 a[168:171], v21, s[12:15], s84 offen offset:1024 sc1\n"
    "v_mfma_f32_16x16x32_bf16 a[44:47], v[204:207], v[48:51], a[44:47]\n"
    "v_mfma_f32_16x16x32_bf16 a[48:51], v[252:255], v[44:47], a[48:51]\n"
    "v_mfma_f32_16x16x32_bf16 a[48:51], v[200:203], v[48:51], a[48:51]\n"
    "s_waitcnt vmcnt(10)\n"
    "v_mfma_f32_16x16x32_bf16 a[52:55], a[184:187], v[52:55], a[52:55]\n"
    "s_waitcnt vmcnt(9)\n"
    "v_mfma_f32_16x16x32_bf16 a[52:55], v[228:231], a[72:75], a[52:55]\n"
    "buffer_load_dwordx4 a[208:211], v21, s[12:15], s84 offen offset:3072 sc1\n"
    "v_mfma_f32_16x16x32_bf16 a[56:59], a[180:183], v[52:55], a[56:59]\n"
    "v_mfma_f32_16x16x32_bf16 a[56:59], v[224:227], a[72:75], a[56:59]\n"
    "v_mfma_f32_16x16x32_bf16 a[60:63], a[176:179], v[52:55], a[60:63]\n"
    "v_mfma_f32_16x16x32_bf16 a[60:63], v[208:211], a[72:75], a[60:63]\n"
    "v_mfma_f32_16x16x32_bf16 a[64:67], a[172:175], v[52:55], a[64:67]\n"
    "buffer_load_dwordx4 a[212:215], v11, s[12:15], s85 offen offset:1024 sc1\n"
    "v_mfma_f32_16x16x32_bf16 a[64:67], v[204:207], a[72:75], a[64:67]\n"
    "v_mfma_f32_16x16x32_bf16 a[68:71], v[252:255], v[52:55], a[68:71]\n"
    "v_mfma_f32_16x16x32_bf16 a[68:71], v[200:203], a[72:75], a[68:71]\n"
    "s_waitcnt vmcnt(10)\n"
    "v_mfma_f32_16x16x32_bf16 v[108:111], a[184:187], a[76:79], v[108:111]\n"
    "s_waitcnt vmcnt(9)\n"
    "v_mfma_f32_16x16x32_bf16 v[108:111], v[228:231], a[80:83], v[108:111]\n"
    "buffer_load_dwordx4 a[72:75], v11, s[12:15], s85 offen offset:3072 sc1\n"
    "v_mfma_f32_16x16x32_bf16 v[112:115], a[180:183], a[76:79], v[112:115]\n"
    "v_mfma_f32_16x16x32_bf16 v[112:115], v[224:227], a[80:83], v[112:115]\n"
    "v_mfma_f32_16x16x32_bf16 v[128:131], a[176:179], a[76:79], v[128:131]\n"
    "v_mfma_f32_16x16x32_bf16 v[128:131], v[208:211], a[80:83], v[128:131]\n"
    "v_mfma_f32_16x16x32_bf16 v[132:135], a[172:175], a[76:79], v[132:135]\n"
    "buffer_load_dwordx4 a[236:239], v16, s[12:15], s85 offen offset:1024 sc1\n"
    "v_mfma_f32_16x16x32_bf16 v[132:135], v[204:207], a[80:83], v[132:135]\n"
    "v_mfma_f32_16x16x32_bf16 v[136:139], v[252:255], a[76:79], v[136:139]\n"
    "v_mfma_f32_16x16x32_bf16 v[136:139], v[200:203], a[80:83], v[136:139]\n"
    "s_waitcnt vmcnt(10)\n"
    "v_mfma_f32_16x16x32_bf16 a[0:3], a[232:235], a[84:87], a[0:3]\n"
    "s_waitcnt vmcnt(9)\n"
    "v_mfma_f32_16x16x32_bf16 a[0:3], a[204:207], a[88:91], a[0:3]\n"
    "buffer_load_dwordx4 a[76:79], v16, s[12:15], s85 offen offset:3072 sc1\n"
    "v_mfma_f32_16x16x32_bf16 a[4:7], a[228:231], a[84:87], a[4:7]\n"
    "v_mfma_f32_16x16x32_bf16 a[4:7], a[200:203], a[88:91], a[4:7]\n"
    "v_mfma_f32_16x16x32_bf16 a[8:11], a[224:227], a[84:87], a[8:11]\n"
    "v_mfma_f32_16x16x32_bf16 a[8:11], a[196:199], a[88:91], a[8:11]\n"
    "v_mfma_f32_16x16x32_bf16 a[12:15], a[220:223], a[84:87], a[12:15]\n"
    "buffer_load_dwordx4 a[80:83], v20, s[12:15], s85 offen offset:1024 sc1\n"
    "v_mfma_f32_16x16x32_bf16 a[12:15], a[192:195], a[88:91], a[12:15]\n"
    "v_mfma_f32_16x16x32_bf16 a[28:31], a[216:219], a[84:87], a[28:31]\n"
    "v_mfma_f32_16x16x32_bf16 a[28:31], a[188:191], a[88:91], a[28:31]\n"
    "s_waitcnt vmcnt(10)\n"
    "v_mfma_f32_16x16x32_bf16 a[32:35], a[232:235], a[164:167], a[32:35]\n"
    "s_waitcnt vmcnt(9)\n"
    "v_mfma_f32_16x16x32_bf16 a[32:35], a[204:207], a[16:19], a[32:35]\n"
    "buffer_load_dwordx4 a[84:87], v20, s[12:15], s85 offen offset:3072 sc1\n"
    "v_mfma_f32_16x16x32_bf16 a[36:39], a[228:231], a[164:167], a[36:39]\n"
    "v_mfma_f32_16x16x32_bf16 a[36:39], a[200:203], a[16:19], a[36:39]\n"
    "v_mfma_f32_16x16x32_bf16 a[40:43], a[224:227], a[164:167], a[40:43]\n"
    "v_mfma_f32_16x16x32_bf16 a[40:43], a[196:199], a[16:19], a[40:43]\n"
    "v_mfma_f32_16x16x32_bf16 a[44:47], a[220:223], a[164:167], a[44:47]\n"
    "buffer_load_dwordx2 v[12:13], v17, s[12:15], s63 offen offset:512 sc1\n"
    "buffer_load_dwordx4 a[88:91], v21, s[12:15], s85 offen offset:1024 sc1\n"
    "v_mfma_f32_16x16x32_bf16 a[44:47], a[192:195], a[16:19], a[44:47]\n"
    "v_mfma_f32_16x16x32_bf16 a[48:51], a[216:219], a[164:167], a[48:51]\n"
    "v_mfma_f32_16x16x32_bf16 a[48:51], a[188:191], a[16:19], a[48:51]\n"
    "s_waitcnt vmcnt(11)\n"
    "v_mfma_f32_16x16x32_bf16 a[52:55], a[232:235], a[20:23], a[52:55]\n"
    "s_waitcnt vmcnt(10)\n"
    "v_mfma_f32_16x16x32_bf16 a[52:55], a[204:207], a[24:27], a[52:55]\n"
    "buffer_load_dwordx4 a[16:19], v21, s[12:15], s85 offen offset:3072 sc1\n"
    "v_mfma_f32_16x16x32_bf16 a[56:59], a[228:231], a[20:23], a[56:59]\n"
    "v_mfma_f32_16x16x32_bf16 a[56:59], a[200:203], a[24:27], a[56:59]\n"
    "v_mfma_f32_16x16x32_bf16 a[60:63], a[224:227], a[20:23], a[60:63]\n"
    "v_mfma_f32_16x16x32_bf16 a[60:63], a[196:199], a[24:27], a[60:63]\n"
    "v_mfma_f32_16x16x32_bf16 a[64:67], a[220:223], a[20:23], a[64:67]\n"
    "buffer_load_dwordx4 a[164:167], v11, s[12:15], s86 offen sc1\n"
    "v_mfma_f32_16x16x32_bf16 a[64:67], a[192:195], a[24:27], a[64:67]\n"
    "v_mfma_f32_16x16x32_bf16 a[68:71], a[216:219], a[20:23], a[68:71]\n"
    "v_mfma_f32_16x16x32_bf16 a[68:71], a[188:191], a[24:27], a[68:71]\n"
    "s_waitcnt vmcnt(11)\n"
    "v_mfma_f32_16x16x32_bf16 v[108:111], a[232:235], a[168:171], v[108:111]\n"
    "s_waitcnt vmcnt(10)\n"
    "v_mfma_f32_16x16x32_bf16 v[108:111], a[204:207], a[208:211], v[108:111]\n"
    "buffer_load_dwordx4 a[20:23], v22, s[12:15], s68 offen sc1\n"
    "v_mfma_f32_16x16x32_bf16 v[112:115], a[228:231], a[168:171], v[112:115]\n"
    "v_mfma_f32_16x16x32_bf16 v[112:115], a[200:203], a[208:211], v[112:115]\n"
    "v_mfma_f32_16x16x32_bf16 v[128:131], a[224:227], a[168:171], v[128:131]\n"
    "v_mfma_f32_16x16x32_bf16 v[128:131], a[196:199], a[208:211], v[128:131]\n"
    "v_mfma_f32_16x16x32_bf16 v[132:135], a[220:223], a[168:171], v[132:135]\n"
    "buffer_load_dwordx4 a[24:27], v16, s[12:15], s86 offen sc1\n"
    "v_mfma_f32_16x16x32_bf16 v[132:135], a[192:195], a[208:211], v[132:135]\n"
    "v_mfma_f32_16x16x32_bf16 v[136:139], a[216:219], a[168:171], v[136:139]\n"
    "v_mfma_f32_16x16x32_bf16 v[136:139], a[188:191], a[208:211], v[136:139]\n"
    "s_waitcnt vmcnt(11)\n"
    "v_mfma_f32_16x16x32_bf16 a[0:3], v[248:251], a[212:215], a[0:3]\n"
    "s_waitcnt vmcnt(10)\n"
    "v_mfma_f32_16x16x32_bf16 a[0:3], v[196:199], a[72:75], a[0:3]\n"
    "buffer_load_dwordx4 a[168:171], v23, s[12:15], s68 offen sc1\n"
    "v_mfma_f32_16x16x32_bf16 a[4:7], v[244:247], a[212:215], a[4:7]\n"
    "v_mfma_f32_16x16x32_bf16 a[4:7], v[192:195], a[72:75], a[4:7]\n"
    "v_mfma_f32_16x16x32_bf16 a[8:11], v[240:243], a[212:215], a[8:11]\n"
    "v_mfma_f32_16x16x32_bf16 a[8:11], v[188:191], a[72:75], a[8:11]\n"
    "v_mfma_f32_16x16x32_bf16 a[12:15], v[236:239], a[212:215], a[12:15]\n"
    "buffer_load_dwordx4 a[172:175], v20, s[12:15], s86 offen sc1\n"
    "v_mfma_f32_16x16x32_bf16 a[12:15], v[184:187], a[72:75], a[12:15]\n"
    "v_mfma_f32_16x16x32_bf16 a[28:31], v[232:235], a[212:215], a[28:31]\n"
    "v_mfma_f32_16x16x32_bf16 a[28:31], v[180:183], a[72:75], a[28:31]\n"
    "s_waitcnt vmcnt(11)\n"
    "v_mfma_f32_16x16x32_bf16 a[32:35], v[248:251], a[236:239], a[32:35]\n"
    "s_waitcnt vmcnt(10)\n"
    "v_mfma_f32_16x16x32_bf16 a[32:35], v[196:199], a[76:79], a[32:35]\n"
    "buffer_load_dwordx4 a[72:75], v24, s[12:15], s68 offen sc1\n"
    "v_mfma_f32_16x16x32_bf16 a[36:39], v[244:247], a[236:239], a[36:39]\n"
    "v_mfma_f32_16x16x32_bf16 a[36:39], v[192:195], a[76:79], a[36:39]\n"
    "v_mfma_f32_16x16x32_bf16 a[40:43], v[240:243], a[236:239], a[40:43]\n"
    "v_mfma_f32_16x16x32_bf16 a[40:43], v[188:191], a[76:79], a[40:43]\n"
    "v_mfma_f32_16x16x32_bf16 a[44:47], v[236:239], a[236:239], a[44:47]\n"
    "buffer_load_dwordx4 a[176:179], v21, s[12:15], s86 offen sc1\n"
    "v_mfma_f32_16x16x32_bf16 a[44:47], v[184:187], a[76:79], a[44:47]\n"
    "v_mfma_f32_16x16x32_bf16 a[48:51], v[232:235], a[236:239], a[48:51]\n"
    "v_mfma_f32_16x16x32_bf16 a[48:51], v[180:183], a[76:79], a[48:51]\n"
    "s_waitcnt vmcnt(11)\n"
    "v_mfma_f32_16x16x32_bf16 a[52:55], v[248:251], a[80:83], a[52:55]\n"
    "s_waitcnt vmcnt(10)\n"
    "v_mfma_f32_16x16x32_bf16 a[52:55], v[196:199], a[84:87], a[52:55]\n"
    "buffer_load_dwordx4 a[76:79], v25, s[12:15], s68 offen sc1\n"
    "v_mfma_f32_16x16x32_bf16 a[56:59], v[244:247], a[80:83], a[56:59]\n"
    "v_mfma_f32_16x16x32_bf16 a[56:59], v[192:195], a[84:87], a[56:59]\n"
    "v_mfma_f32_16x16x32_bf16 a[60:63], v[240:243], a[80:83], a[60:63]\n"
    "v_mfma_f32_16x16x32_bf16 a[60:63], v[188:191], a[84:87], a[60:63]\n"
    "v_mfma_f32_16x16x32_bf16 a[64:67], v[236:239], a[80:83], a[64:67]\n"
    "s_waitcnt vmcnt(10)\n"
    "v_cmp_ne_u64 vcc, s[38:39], v[12:13]\n"
    "s_nop 0\n"
    "s_and_b64 vcc, vcc, s[28:29]\n"
    "s_cbranch_scc0 .Lok_35_%=\n"
    "s_movk_i32 s46, 0x4e20\n"
    ".Lspin_34_%=:\n"
    "buffer_load_dwordx2 v[12:13], v17, s[12:15], s63 offen offset:512 sc1\n"
    "s_sub_u32 s46, s46, 1\n"
    "s_cbranch_scc1 .Lok_35_%=\n"
    "s_waitcnt vmcnt(0)\n"
    "v_cmp_ne_u64 vcc, s[38:39], v[12:13]\n"
    "s_nop 0\n"
    "s_and_b64 vcc, vcc, s[28:29]\n"
    "s_cbranch_scc1 .Lspin_34_%=\n"
    ".Lok_35_%=:\n"
    "buffer_load_dwordx4 a[180:183], v11, s[12:15], s87 offen sc1\n"
    "v_mfma_f32_16x16x32_bf16 a[64:67], v[184:187], a[84:87], a[64:67]\n"
    "v_mfma_f32_16x16x32_bf16 a[68:71], v[232:235], a[80:83], a[68:71]\n"
    "v_mfma_f32_16x16x32_bf16 a[68:71], v[180:183], a[84:87], a[68:71]\n"
    "s_waitcnt vmcnt(10)\n"
    "v_mfma_f32_16x16x32_bf16 v[108:111], v[248:251], a[88:91], v[108:111]\n"
    "s_waitcnt vmcnt(9)\n"
    "v_mfma_f32_16x16x32_bf16 v[108:111], v[196:199], a[16:19], v[108:111]\n"
    "buffer_load_dwordx4 a[80:83], v11, s[12:15], s87 offen offset:1024 sc1\n"
    "v_mfma_f32_16x16x32_bf16 v[112:115], v[244:247], a[88:91], v[112:115]\n"
    "v_mfma_f32_16x16x32_bf16 v[112:115], v[192:195], a[16:19], v[112:115]\n"
    "v_mfma_f32_16x16x32_bf16 v[128:131], v[240:243], a[88:91], v[128:131]\n"
    "v_mfma_f32_16x16x32_bf16 v[128:131], v[188:191], a[16:19], v[128:131]\n"
    "v_mfma_f32_16x16x32_bf16 v[132:135], v[236:239], a[88:91], v[132:135]\n"
    "buffer_load_dwordx4 a[84:87], v11, s[12:15], s87 offen offset:2048 sc1\n"
    "v_mfma_f32_16x16x32_bf16 v[132:135], v[184:187], a[16:19], v[132:135]\n"
    "v_mfma_f32_16x16x32_bf16 v[136:139], v[232:235], a[88:91], v[136:139]\n"
    "v_mfma_f32_16x16x32_bf16 v[136:139], v[180:183], a[16:19], v[136:139]\n"
    "s_waitcnt vmcnt(10)\n"
    "v_mfma_f32_16x16x32_bf16 a[0:3], v[176:179], a[164:167], a[0:3]\n"
    "s_waitcnt vmcnt(9)\n"
    "v_mfma_f32_16x16x32_bf16 a[0:3], v[156:159], a[20:23], a[0:3]\n"
    "buffer_load_dwordx4 a[16:19], v11, s[12:15], s87 offen offset:3072 sc1\n"
    "v_mfma_f32_16x16x32_bf16 a[4:7], v[172:175], a[164:167], a[4:7]\n"
    "v_mfma_f32_16x16x32_bf16 a[4:7], v[152:155], a[20:23], a[4:7]\n"
    "v_mfma_f32_16x16x32_bf16 a[8:11], v[168:171], a[164:167], a[8:11]\n"
    "v_mfma_f32_16x16x32_bf16 a[8:11], v[148:151], a[20:23], a[8:11]\n"
    "v_mfma_f32_16x16x32_bf16 a[12:15], v[164:167], a[164:167], a[12:15]\n"
    "buffer_load_dwordx4 a[88:91], v16, s[12:15], s87 offen sc1\n"
    "v_mfma_f32_16x16x32_bf16 a[12:15], v[76:79], a[20:23], a[12:15]\n"
    "v_mfma_f32_16x16x32_bf16 a[28:31], v[160:163], a[164:167], a[28:31]\n"
    "v_mfma_f32_16x16x32_bf16 a[28:31], v[72:75], a[20:23], a[28:31]\n"
    "s_waitcnt vmcnt(10)\n"
    "v_mfma_f32_16x16x32_bf16 a[32:35], v[176:179], a[24:27], a[32:35]\n"
    "s_waitcnt vmcnt(9)\n"
    "v_mfma_f32_16x16x32_bf16 a[32:35], v[156:159], a[168:171], a[32:35]\n"
    "buffer_load_dwordx4 a[20:23], v16, s[12:15], s87 offen offset:1024 sc1\n"
    "v_mfma_f32_16x16x32_bf16 a[36:39], v[172:175], a[24:27], a[36:39]\n"
    "v_mfma_f32_16x16x32_bf16 a[36:39], v[152:155], a[168:171], a[36:39]\n"
    "v_mfma_f32_16x16x32_bf16 a[40:43], v[168:171], a[24:27], a[40:43]\n"
    "v_mfma_f32_16x16x32_bf16 a[40:43], v[148:151], a[168:171], a[40:43]\n"
    "v_mfma_f32_16x16x32_bf16 a[44:47], v[164:167], a[24:27], a[44:47]\n"
    "buffer_load_dwordx4 a[164:167], v16, s[12:15], s87 offen offset:2048 sc1\n"
    "v_mfma_f32_16x16x32_bf16 a[44:47], v[76:79], a[168:171], a[44:47]\n"
    "v_mfma_f32_16x16x32_bf16 a[48:51], v[160:163], a[24:27], a[48:51]\n"
    "v_mfma_f32_16x16x32_bf16 a[48:51], v[72:75], a[168:171], a[48:51]\n"
    "s_waitcnt vmcnt(10)\n"
    "v_mfma_f32_16x16x32_bf16 a[52:55], v[176:179], a[172:175], a[52:55]\n"
    "s_waitcnt vmcnt(9)\n"
    "v_mfma_f32_16x16x32_bf16 a[52:55], v[156:159], a[72:75], a[52:55]\n"
    "buffer_load_dwordx4 a[24:27], v16, s[12:15], s87 offen offset:3072 sc1\n"
    "v_mfma_f32_16x16x32_bf16 a[56:59], v[172:175], a[172:175], a[56:59]\n"
    "v_mfma_f32_16x16x32_bf16 a[56:59], v[152:155], a[72:75], a[56:59]\n"
    "v_mfma_f32_16x16x32_bf16 a[60:63], v[168:171], a[172:175], a[60:63]\n"
    "v_mfma_f32_16x16x32_bf16 a[60:63], v[148:151], a[72:75], a[60:63]\n"
    "v_mfma_f32_16x16x32_bf16 a[64:67], v[164:167], a[172:175], a[64:67]\n"
    "buffer_load_dwordx4 a[168:171], v20, s[12:15], s87 offen sc1\n"
    "v_mfma_f32_16x16x32_bf16 a[64:67], v[76:79], a[72:75], a[64:67]\n"
    "v_mfma_f32_16x16x32_bf16 a[68:71], v[160:163], a[172:175], a[68:71]\n"
    "v_mfma_f32_16x16x32_bf16 a[68:71], v[72:75], a[72:75], a[68:71]\n"
    "s_waitcnt vmcnt(10)\n"
    "v_mfma_f32_16x16x32_bf16 v[108:111], v[176:179], a[176:179], v[108:111]\n"
    "s_waitcnt vmcnt(9)\n"
    "v_mfma_f32_16x16x32_bf16 v[108:111], v[156:159], a[76:79], v[108:111]\n"
    "buffer_load_dwordx4 a[72:75], v20, s[12:15], s87 offen offset:1024 sc1\n"
    "v_mfma_f32_16x16x32_bf16 v[112:115], v[172:175], a[176:179], v[112:115]\n"
    "v_mfma_f32_16x16x32_bf16 v[112:115], v[152:155], a[76:79], v[112:115]\n"
    "v_mfma_f32_16x16x32_bf16 v[128:131], v[168:171], a[176:179], v[128:131]\n"
    "v_mfma_f32_16x16x32_bf16 v[128:131], v[148:151], a[76:79], v[128:131]\n"
    "v_mfma_f32_16x16x32_bf16 v[132:135], v[164:167], a[176:179], v[132:135]\n"
    "buffer_load_dwordx4 a[172:175], v20, s[12:15], s87 offen offset:2048 sc1\n"
    "v_mfma_f32_16x16x32_bf16 v[132:135], v[76:79], a[76:79], v[132:135]\n"
    "v_mfma_f32_16x16x32_bf16 v[136:139], v[160:163], a[176:179], v[136:139]\n"
    "v_mfma_f32_16x16x32_bf16 v[136:139], v[72:75], a[76:79], v[136:139]\n"
    "s_waitcnt vmcnt(10)\n"
    "v_mfma_f32_16x16x32_bf16 a[0:3], v[116:119], a[180:183], a[0:3]\n"
    "s_waitcnt vmcnt(9)\n"
    "v_mfma_f32_16x16x32_bf16 a[0:3], a[100:103], a[80:83], a[0:3]\n"
    "buffer_load_dwordx4 a[76:79], v20, s[12:15], s87 offen offset:3072 sc1\n"
    "s_waitcnt vmcnt(9)\n"
    "v_mfma_f32_16x16x32_bf16 a[0:3], a[128:131], a[84:87], a[0:3]\n"
    "s_waitcnt vmcnt(8)\n"
    "v_mfma_f32_16x16x32_bf16 a[0:3], a[140:143], a[16:19], a[0:3]\n"
    "v_mfma_f32_16x16x32_bf16 a[4:7], v[88:91], a[180:183], a[4:7]\n"
    "v_mfma_f32_16x16x32_bf16 a[4:7], a[112:115], a[80:83], a[4:7]\n"
    "buffer_load_dwordx4 a[176:179], v21, s[12:15], s87 offen sc1\n"
    "v_mfma_f32_16x16x32_bf16 a[4:7], a[124:127], a[84:87], a[4:7]\n"
    "v_mfma_f32_16x16x32_bf16 a[4:7], a[152:155], a[16:19], a[4:7]\n"
    "s_nop 2\n"
    "ds_write_b128 v30, a[0:3]\n"
    "v_mfma_f32_16x16x32_bf16 a[8:11], a[96:99], a[180:183], a[8:11]\n"
    "v_mfma_f32_16x16x32_bf16 a[8:11], a[108:111], a[80:83], a[8:11]\n"
    "buffer_load_dwordx4 a[184:187], v21, s[12:15], s87 offen offset:1024 sc1\n"
    "v_mfma_f32_16x16x32_bf16 a[8:11], a[136:139], a[84:87], a[8:11]\n"
    "v_mfma_f32_16x16x32_bf16 a[8:11], a[148:151], a[16:19], a[8:11]\n"
    "ds_write_b128 v30, a[4:7] offset:1024\n"
    "v_mfma_f32_16x16x32_bf16 a[12:15], a[92:95], a[180:183], a[12:15]\n"
    "v_mfma_f32_16x16x32_bf16 a[12:15], a[120:123], a[80:83], a[12:15]\n"
    "buffer_load_dwordx4 a[188:191], v21, s[12:15], s87 offen offset:2048 sc1\n"
    "v_mfma_f32_16x16x32_bf16 a[12:15], a[132:135], a[84:87], a[12:15]\n"
    "v_mfma_f32_16x16x32_bf16 a[12:15], a[160:163], a[16:19], a[12:15]\n"
    "s_nop 1\n"
    "ds_write_b128 v30, a[8:11] offset:2048\n"
    "v_mfma_f32_16x16x32_bf16 a[28:31], a[104:107], a[180:183], a[28:31]\n"
    "v_mfma_f32_16x16x32_bf16 a[28:31], a[116:119], a[80:83], a[28:31]\n"
    "buffer_load_dwordx4 a[192:195], v21, s[12:15], s87 offen offset:3072 sc1\n"
    "v_mfma_f32_16x16x32_bf16 a[28:31], a[144:147], a[84:87], a[28:31]\n"
    "v_mfma_f32_16x16x32_bf16 a[28:31], a[156:159], a[16:19], a[28:31]\n"
    "ds_write_b128 v30, a[12:15] offset:3072\n"
    "s_waitcnt vmcnt(11)\n"
    "v_mfma_f32_16x16x32_bf16 a[32:35], v[116:119], a[88:91], a[32:35]\n"
    "s_waitcnt vmcnt(10)\n"
    "v_mfma_f32_16x16x32_bf16 a[32:35], a[100:103], a[20:23], a[32:35]\n"
    "s_waitcnt vmcnt(9)\n"
    "v_mfma_f32_16x16x32_bf16 a[32:35], a[128:131], a[164:167], a[32:35]\n"
    "s_waitcnt vmcnt(8)\n"
    "v_mfma_f32_16x16x32_bf16 a[32:35], a[140:143], a[24:27], a[32:35]\n"
    "ds_write_b128 v30, a[28:31] offset:4096\n"
    "v_mfma_f32_16x16x32_bf16 a[36:39], v[88:91], a[88:91], a[36:39]\n"
    "v_mfma_f32_16x16x32_bf16 a[36:39], a[112:115], a[20:23], a[36:39]\n"
    "v_mfma_f32_16x16x32_bf16 a[36:39], a[124:127], a[164:167], a[36:39]\n"
    "v_mfma_f32_16x16x32_bf16 a[36:39], a[152:155], a[24:27], a[36:39]\n"
    "s_nop 2\n"
    "ds_write_b128 v31, a[32:35]\n"
    "v_mfma_f32_16x16x32_bf16 a[40:43], a[96:99], a[88:91], a[40:43]\n"
    "s_waitcnt lgkmcnt(1)\n"
    "s_barrier\n"
    "ds_read_b128 v[32:35], v81\n"
    "v_mfma_f32_16x16x32_bf16 a[40:43], a[108:111], a[20:23], a[40:43]\n"
    "ds_read_b128 v[36:39], v81 offset:1024\n"
    "v_mfma_f32_16x16x32_bf16 a[40:43], a[136:139], a[164:167], a[40:43]\n"
    "ds_read_b128 v[40:43], v81 offset:2048\n"
    "v_mfma_f32_16x16x32_bf16 a[40:43], a[148:151], a[24:27], a[40:43]\n"
    "ds_read_b128 v[44:47], v81 offset:3072\n"
    "ds_write_b128 v31, a[36:39] offset:1024\n"
    "v_mfma_f32_16x16x32_bf16 a[44:47], a[92:95], a[88:91], a[44:47]\n"
    "ds_read_b128 v[48:51], v81 offset:4096\n"
    "v_mfma_f32_16x16x32_bf16 a[44:47], a[120:123], a[20:23], a[44:47]\n"
    "v_mfma_f32_16x16x32_bf16 a[44:47], a[132:135], a[164:167], a[44:47]\n"
    "s_waitcnt lgkmcnt(5)\n"
    "v_add_f32 v108, v108, v32\n"
    "v_add_f32 v109, v109, v33\n"
    "v_mfma_f32_16x16x32_bf16 a[44:47], a[160:163], a[24:27], a[44:47]\n"
    "ds_write_b128 v31, a[40:43] offset:2048\n"
    "v_add_f32 v110, v110, v34\n"
    "v_add_f32 v111, v111, v35\n"
    "v_mfma_f32_16x16x32_bf16 a[48:51], a[104:107], a[88:91], a[48:51]\n"
    "s_waitcnt lgkmcnt(5)\n"
    "v_add_f32 v112, v112, v36\n"
    "v_add_f32 v113, v113, v37\n"
    "v_mfma_f32_16x16x32_bf16 a[48:51], a[116:119], a[20:23], a[48:51]\n"
    "v_add_f32 v114, v114, v38\n"
    "v_add_f32 v115, v115, v39\n"
    "v_mfma_f32_16x16x32_bf16 a[48:51], a[144:147], a[164:167], a[48:51]\n"
    "s_waitcnt lgkmcnt(4)\n"
    "v_add_f32 v128, v128, v40\n"
    "v_add_f32 v129, v129, v41\n"
    "v_mfma_f32_16x16x32_bf16 a[48:51], a[156:159], a[24:27], a[48:51]\n"
    "ds_write_b128 v31, a[44:47] offset:3072\n"
    "v_add_f32 v130, v130, v42\n"
    "v_add_f32 v131, v131, v43\n"
    "s_waitcnt vmcnt(7)\n"
    "v_mfma_f32_16x16x32_bf16 a[52:55], v[116:119], a[168:171], a[52:55]\n"
    "s_waitcnt vmcnt(6)\n"
    "s_waitcnt lgkmcnt(4)\n"
    "v_add_f32 v132, v132, v44\n"
    "v_mfma_f32_16x16x32_bf16 a[52:55], a[100:103], a[72:75], a[52:55]\n"
    "s_waitcnt vmcnt(5)\n"
    "v_add_f32 v133, v133, v45\n"
    "v_add_f32 v134, v134, v46\n"
    "v_mfma_f32_16x16x32_bf16 a[52:55], a[128:131], a[172:175], a[52:55]\n"
    "s_waitcnt vmcnt(4)\n"
    "v_add_f32 v135, v135, v47\n"
    "s_waitcnt lgkmcnt(2)\n"
    "v_add_f32 v136, v136, v48\n"
    "v_mfma_f32_16x16x32_bf16 a[52:55], a[140:143], a[76:79], a[52:55]\n"
    "ds_write_b128 v31, a[48:51] offset:4096\n"
    "v_add_f32 v137, v137, v49\n"
    "v_add_f32 v138, v138, v50\n"
    "v_mfma_f32_16x16x32_bf16 a[56:59], v[88:91], a[168:171], a[56:59]\n"
    "v_add_f32 v139, v139, v51\n"
    "v_mfma_f32_16x16x32_bf16 a[56:59], a[112:115], a[72:75], a[56:59]\n"
    "v_mfma_f32_16x16x32_bf16 a[56:59], a[124:127], a[172:175], a[56:59]\n"
    "v_mfma_f32_16x16x32_bf16 a[56:59], a[152:155], a[76:79], a[56:59]\n"
    "ds_write_b128 v80, a[52:55]\n"
    "v_mfma_f32_16x16x32_bf16 a[60:63], a[96:99], a[168:171], a[60:63]\n"
    "s_waitcnt lgkmcnt(1)\n"
    "s_barrier\n"
    "ds_read_b128 v[52:55], v81 offset:5120\n"
    "v_mfma_f32_16x16x32_bf16 a[60:63], a[108:111], a[72:75], a[60:63]\n"
    "ds_read_b128 v[56:59], v81 offset:6144\n"
    "v_mfma_f32_16x16x32_bf16 a[60:63], a[136:139], a[172:175], a[60:63]\n"
    "ds_read_b128 v[60:63], v81 offset:7168\n"
    "v_mfma_f32_16x16x32_bf16 a[60:63], a[148:151], a[76:79], a[60:63]\n"
    "ds_read_b128 v[64:67], v81 offset:8192\n"
    "ds_write_b128 v80, a[56:59] offset:1024\n"
    "v_mfma_f32_16x16x32_bf16 a[64:67], a[92:95], a[168:171], a[64:67]\n"
    "ds_read_b128 v[68:71], v81 offset:9216\n"
    "v_mfma_f32_16x16x32_bf16 a[64:67], a[120:123], a[72:75], a[64:67]\n"
    "v_mfma_f32_16x16x32_bf16 a[64:67], a[132:135], a[172:175], a[64:67]\n"
    "s_waitcnt lgkmcnt(5)\n"
    "v_add_f32 v108, v108, v52\n"
    "v_add_f32 v109, v109, v53\n"
    "v_mfma_f32_16x16x32_bf16 a[64:67], a[160:163], a[76:79], a[64:67]\n"
    "ds_write_b128 v80, a[60:63] offset:2048\n"
    "v_add_f32 v110, v110, v54\n"
    "v_add_f32 v111, v111, v55\n"
    "v_mfma_f32_16x16x32_bf16 a[68:71], a[104:107], a[168:171], a[68:71]\n"
    "s_waitcnt lgkmcnt(5)\n"
    "v_add_f32 v112, v112, v56\n"
    "v_add_f32 v113, v113, v57\n"
    "v_mfma_f32_16x16x32_bf16 a[68:71], a[116:119], a[72:75], a[68:71]\n"
    "v_add_f32 v114, v114, v58\n"
    "v_add_f32 v115, v115, v59\n"
    "v_mfma_f32_16x16x32_bf16 a[68:71], a[144:147], a[172:175], a[68:71]\n"
    "s_waitcnt lgkmcnt(4)\n"
    "v_add_f32 v128, v128, v60\n"
    "v_add_f32 v129, v129, v61\n"
    "v_mfma_f32_16x16x32_bf16 a[68:71], a[156:159], a[76:79], a[68:71]\n"
    "ds_write_b128 v80, a[64:67] offset:3072\n"
    "v_add_f32 v130, v130, v62\n"
    "v_add_f32 v131, v131, v63\n"
    "s_waitcnt vmcnt(3)\n"
    "v_mfma_f32_16x16x32_bf16 v[108:111], v[116:119], a[176:179], v[108:111]\n"
    "s_waitcnt vmcnt(2)\n"
    "s_waitcnt lgkmcnt(4)\n"
    "v_add_f32 v132, v132, v64\n"
    "v_mfma_f32_16x16x32_bf16 v[108:111], a[100:103], a[184:187], v[108:111]\n"
    "s_waitcnt vmcnt(1)\n"
    "v_add_f32 v133, v133, v65\n"
    "v_add_f32 v134, v134, v66\n"
    "v_mfma_f32_16x16x32_bf16 v[108:111], a[128:131], a[188:191], v[108:111]\n"
    "s_waitcnt vmcnt(0)\n"
    "v_add_f32 v135, v135, v67\n"
    "s_waitcnt lgkmcnt(2)\n"
    "v_add_f32 v136, v136, v68\n"
    "v_mfma_f32_16x16x32_bf16 v[108:111], a[140:143], a[192:195], v[108:111]\n"
    "ds_write_b128 v80, a[68:71] offset:4096\n"
    "v_add_f32 v137, v137, v69\n"
    "v_add_f32 v138, v138, v70\n"
    "v_mfma_f32_16x16x32_bf16 v[112:115], v[88:91], a[176:179], v[112:115]\n"
    "v_add_f32 v139, v139, v71\n"
    "v_mfma_f32_16x16x32_bf16 v[112:115], a[112:115], a[184:187], v[112:115]\n"
    "v_mfma_f32_16x16x32_bf16 v[112:115], a[124:127], a[188:191], v[112:115]\n"
    "v_mfma_f32_16x16x32_bf16 v[112:115], a[152:155], a[192:195], v[112:115]\n"
    "v_mfma_f32_16x16x32_bf16 v[128:131], a[96:99], a[176:179], v[128:131]\n"
    "s_waitcnt lgkmcnt(0)\n"
    "s_barrier\n"
    "ds_read_b128 v[32:35], v81 offset:10240\n"
    "v_mfma_f32_16x16x32_bf16 v[128:131], a[108:111], a[184:187], v[128:131]\n"
    "ds_read_b128 v[36:39], v81 offset:11264\n"
    "v_mfma_f32_16x16x32_bf16 v[128:131], a[136:139], a[188:191], v[128:131]\n"
    "ds_read_b128 v[40:43], v81 offset:12288\n"
    "v_mfma_f32_16x16x32_bf16 v[128:131], a[148:151], a[192:195], v[128:131]\n"
    "ds_read_b128 v[44:47], v81 offset:13312\n"
    "v_mfma_f32_16x16x32_bf16 v[132:135], a[92:95], a[176:179], v[132:135]\n"
    "ds_read_b128 v[48:51], v81 offset:14336\n"
    "v_mfma_f32_16x16x32_bf16 v[132:135], a[120:123], a[184:187], v[132:135]\n"
    "s_waitcnt lgkmcnt(4)\n"
    "v_add_f32 v108, v108, v32\n"
    "v_add_f32 v109, v109, v33\n"
    "v_mfma_f32_16x16x32_bf16 v[132:135], a[132:135], a[188:191], v[132:135]\n"
    "v_add_f32 v110, v110, v34\n"
    "v_add_f32 v111, v111, v35\n"
    "v_mfma_f32_16x16x32_bf16 v[132:135], a[160:163], a[192:195], v[132:135]\n"
    "v_cvt_pk_bf16_f32 v212, v108, v109\n"
    "v_cvt_pk_bf16_f32 v213, v110, v111\n"
    "v_mfma_f32_16x16x32_bf16 v[136:139], a[104:107], a[176:179], v[136:139]\n"
    "s_waitcnt lgkmcnt(3)\n"
    "v_add_f32 v112, v112, v36\n"
    "v_add_f32 v113, v113, v37\n"
    "v_mfma_f32_16x16x32_bf16 v[136:139], a[116:119], a[184:187], v[136:139]\n"
    "v_add_f32 v114, v114, v38\n"
    "v_add_f32 v115, v115, v39\n"
    "v_mfma_f32_16x16x32_bf16 v[136:139], a[144:147], a[188:191], v[136:139]\n"
    "v_cvt_pk_bf16_f32 v214, v112, v113\n"
    "v_cvt_pk_bf16_f32 v215, v114, v115\n"
    "v_mfma_f32_16x16x32_bf16 v[136:139], a[156:159], a[192:195], v[136:139]\n"
    "ds_write_b128 v82, v[212:215]\n"
    "ds_read_b128 v[72:75], v84\n"
    "s_waitcnt lgkmcnt(4)\n"
    "v_pk_add_f32 v[128:129], v[128:129], v[40:41]\n"
    "v_pk_add_f32 v[130:131], v[130:131], v[42:43]\n"
    "v_cvt_pk_bf16_f32 v216, v128, v129\n"
    "v_cvt_pk_bf16_f32 v217, v130, v131\n"
    "s_waitcnt lgkmcnt(3)\n"
    "v_pk_add_f32 v[132:133], v[132:133], v[44:45]\n"
    "v_pk_add_f32 v[134:135], v[134:135], v[46:47]\n"
    "v_cvt_pk_bf16_f32 v218, v132, v133\n"
    "v_cvt_pk_bf16_f32 v219, v134, v135\n"
    "ds_write_b128 v82, v[216:219] offset:1024\n"
    "ds_read_b128 v[76:79], v84 offset:1024\n"
    "s_waitcnt lgkmcnt(2)\n"
    "buffer_store_dwordx4 v[72:75], v27, s[16:19], s65 offen\n"
    "v_pk_add_f32 v[136:137], v[136:137], v[48:49]\n"
    "v_pk_add_f32 v[138:139], v[138:139], v[50:51]\n"
    "v_cvt_pk_bf16_f32 v220, v136, v137\n"
    "v_cvt_pk_bf16_f32 v221, v138, v139\n"
    "ds_write_b64 v83, v[220:221] offset:2048\n"
    "ds_read_b128 v[92:95], v85 offset:2048\n"
    "s_waitcnt lgkmcnt(2)\n"
    "buffer_store_dwordx4 v[76:79], v27, s[16:19], s65 offen offset:64\n"
    "s_waitcnt lgkmcnt(0)\n"
    "buffer_store_dwordx4 v[92:95], v28, s[16:19], s62 offen\n"
    ".Lend_1_%=:\n"
    : "+v"(tid)
    : "s"(__builtin_amdgcn_kernarg_segment_ptr()), "s"(blockIdx.x), "s"((unsigned)(size_t)(__attribute__((address_space(3))) char*)lds)
    :
      "v1", "v2", "v3", "v4", "v5", "v6", "v7", "v8", "v9", "v10", "v11", "v12", "v13", "v14", "v15", "v16", "v17", "v18", "v19", "v20", "v21", "v22", "v23", "v24",
      "v25", "v26", "v27", "v28", "v29", "v30", "v31", "v32", "v33", "v34", "v35", "v36", "v37", "v38", "v39", "v40", "v41", "v42", "v43", "v44", "v45", "v46", "v47", "v48",
      "v49", "v50", "v51", "v52", "v53", "v54", "v55", "v56", "v57", "v58", "v59", "v60", "v61", "v62", "v63", "v64", "v65", "v66", "v67", "v68", "v69", "v70", "v71", "v72",
      "v73", "v74", "v75", "v76", "v77", "v78", "v79", "v80", "v81", "v82", "v83", "v84", "v85", "v86", "v87", "v88", "v89", "v90", "v91", "v92", "v93", "v94", "v95", "v96",
      "v97", "v98", "v99", "v100", "v101", "v102", "v103", "v104", "v105", "v106", "v107", "v108", "v109", "v110", "v111", "v112", "v113", "v114", "v115", "v116", "v117", "v118", "v119", "v120",
      "v121", "v122", "v123", "v124", "v125", "v126", "v127", "v128", "v129", "v130", "v131", "v132", "v133", "v134", "v135", "v136", "v137", "v138", "v139", "v140", "v141", "v142", "v143", "v144",
      "v145", "v146", "v147", "v148", "v149", "v150", "v151", "v152", "v153", "v154", "v155", "v156", "v157", "v158", "v159", "v160", "v161", "v162", "v163", "v164", "v165", "v166", "v167", "v168",
      "v169", "v170", "v171", "v172", "v173", "v174", "v175", "v176", "v177", "v178", "v179", "v180", "v181", "v182", "v183", "v184", "v185", "v186", "v187", "v188", "v189", "v190", "v191", "v192",
      "v193", "v194", "v195", "v196", "v197", "v198", "v199", "v200", "v201", "v202", "v203", "v204", "v205", "v206", "v207", "v208", "v209", "v210", "v211", "v212", "v213", "v214", "v215", "v216",
      "v217", "v218", "v219", "v220", "v221", "v222", "v223", "v224", "v225", "v226", "v227", "v228", "v229", "v230", "v231", "v232", "v233", "v234", "v235", "v236", "v237", "v238", "v239", "v240",
      "v241", "v242", "v243", "v244", "v245", "v246", "v247", "v248", "v249", "v250", "v251", "v252", "v253", "v254", "v255", "a0", "a1", "a2", "a3", "a4", "a5", "a6", "a7", "a8",
      "a9", "a10", "a11", "a12", "a13", "a14", "a15", "a16", "a17", "a18", "a19", "a20", "a21", "a22", "a23", "a24", "a25", "a26", "a27", "a28", "a29", "a30", "a31", "a32",
      "a33", "a34", "a35", "a36", "a37", "a38", "a39", "a40", "a41", "a42", "a43", "a44", "a45", "a46", "a47", "a48", "a49", "a50", "a51", "a52", "a53", "a54", "a55", "a56",
      "a57", "a58", "a59", "a60", "a61", "a62", "a63", "a64", "a65", "a66", "a67", "a68", "a69", "a70", "a71", "a72", "a73", "a74", "a75", "a76", "a77", "a78", "a79", "a80",
      "a81", "a82", "a83", "a84", "a85", "a86", "a87", "a88", "a89", "a90", "a91", "a92", "a93", "a94", "a95", "a96", "a97", "a98", "a99", "a100", "a101", "a102", "a103", "a104",
      "a105", "a106", "a107", "a108", "a109", "a110", "a111", "a112", "a113", "a114", "a115", "a116", "a117", "a118", "a119", "a120", "a121", "a122", "a123", "a124", "a125", "a126", "a127", "a128",
      "a129", "a130", "a131", "a132", "a133", "a134", "a135", "a136", "a137", "a138", "a139", "a140", "a141", "a142", "a143", "a144", "a145", "a146", "a147", "a148", "a149", "a150", "a151", "a152",
      "a153", "a154", "a155", "a156", "a157", "a158", "a159", "a160", "a161", "a162", "a163", "a164", "a165", "a166", "a167", "a168", "a169", "a170", "a171", "a172", "a173", "a174", "a175", "a176",
      "a177", "a178", "a179", "a180", "a181", "a182", "a183", "a184", "a185", "a186", "a187", "a188", "a189", "a190", "a191", "a192", "a193", "a194", "a195", "a196", "a197", "a198", "a199", "a200",
      "a201", "a202", "a203", "a204", "a205", "a206", "a207", "a208", "a209", "a210", "a211", "a212", "a213", "a214", "a215", "a216", "a217", "a218", "a219", "a220", "a221", "a222", "a223", "a224",
      "a225", "a226", "a227", "a228", "a229", "a230", "a231", "a232", "a233", "a234", "a235", "a236", "a237", "a238", "a239", "a240", "a241", "a242", "a243", "a244", "a245", "a246", "a247", "a248",
      "a249", "a250", "a251", "a252", "a253", "a254", "a255", "s4", "s5", "s6", "s7", "s8", "s9", "s10", "s11", "s12", "s13", "s14", "s15", "s16", "s17", "s18", "s19", "s20",
      "s21", "s22", "s23", "s24", "s25", "s26", "s27", "s28", "s29", "s30", "s31", "s33", "s34", "s35", "s36", "s37", "s38", "s39", "s40", "s41", "s42", "s43", "s44", "s45",
      "s46", "s47", "s48", "s49", "s50", "s51", "s52", "s53", "s54", "s55", "s56", "s57", "s58", "s59", "s60", "s61", "s62", "s63", "s64", "s65", "s66", "s67", "s68", "s69",
      "s70", "s71", "s72", "s73", "s74", "s75", "s76", "s77", "s78", "s79", "s80", "s81", "s82", "s83", "s84", "s85", "s86", "s87", "s88", "s89", "s90", "s91", "s92", "s93",
      "s94", "s95", "s96", "s97", "s98", "s99", "vcc", "scc", "memory");
}

extern "C" void kernel_launch(void* const* d_in, const int* in_sizes, int n_in, void* d_out, int out_size, void* d_ws, size_t ws_size,
                              hipStream_t stream) {
  fused_block_kernel<<<256, 256, 0, stream>>>(d_in[0], d_in[1], d_in[2], d_in[4], d_in[5], d_out, d_ws);
}
```
